```python
import torch

def _softmax(x, axis=-1):
    return torch.softmax(x, dim=axis)
def _gelu(x):
    return torch.nn.functional.gelu(x, approximate="tanh")
def _silu(x):
    return torch.nn.functional.silu(x)
def _sigmoid(x):
    return torch.sigmoid(x)
def _layernorm(x, axis=-1, eps=1e-5):
    m = x.mean(dim=axis, keepdim=True)
    v = x.var(dim=axis, keepdim=True, unbiased=False)
    return (x - m) / torch.sqrt(v + eps)
def _scatter_add(tgt, idx, src, axis=0):
    return tgt.clone().index_add_(axis, idx, src)
def _segment_sum(src, seg, n):
    out = torch.zeros((n,) + src.shape[1:], dtype=src.dtype)
    return out.index_add_(0, seg, src)
def _pad(x, pw):
    flat = tuple(v for pair in reversed(pw) for v in pair)
    return torch.nn.functional.pad(x, flat)

kernel_name = 'parallel_sum__grouped_kv_mqa__swiglu_ffn__prompt'

def setup_inputs():

    _w = (lambda a: (lambda t: t.to(torch.bfloat16) if t.is_floating_point() else t)(torch.as_tensor(a)))
    def _A_setup():
        B, S, D, H = 1, 2048, 640, 32
        Dh = D // H
        return {
            "x": _w((lambda *s: torch.randn(*s, dtype=torch.bfloat16) * 0.05)(B, S, D)),
            "wq": _w((lambda *s: torch.randn(*s, dtype=torch.bfloat16) * 0.2)(D, H * Dh)),
            "wk": _w((lambda *s: torch.randn(*s, dtype=torch.bfloat16) * 0.2)(D, Dh)),
            "wv": _w((lambda *s: torch.randn(*s, dtype=torch.bfloat16) * 0.1)(D, Dh)),
            "wo": _w((lambda *s: torch.randn(*s, dtype=torch.bfloat16) * 0.1)(H * Dh, D)),
        }
    _A_in = _A_setup()

    def _B_setup():
        B, S, D, F = 1, 2048, 640, 1728
        return {
            "x": _w((lambda *s: torch.randn(*s, dtype=torch.bfloat16) * 0.05)(B, S, D)),
            "w_gate": _w((lambda *s: torch.randn(*s, dtype=torch.bfloat16) * 0.1)(D, F)),
            "w_up": _w((lambda *s: torch.randn(*s, dtype=torch.bfloat16) * 0.1)(D, F)),
            "w_down": _w((lambda *s: torch.randn(*s, dtype=torch.bfloat16) * 0.1)(F, D)),
        }
    _B_in = _B_setup()

    return {
        "A_x": _A_in["x"],
        "A_wq": _A_in["wq"],
        "A_wk": _A_in["wk"],
        "A_wv": _A_in["wv"],
        "A_wo": _A_in["wo"],
        "B_x": _B_in["x"],
        "B_w_gate": _B_in["w_gate"],
        "B_w_up": _B_in["w_up"],
        "B_w_down": _B_in["w_down"],
    }

def reference(A_x, A_wq, A_wk, A_wv, A_wo, B_x, B_w_gate, B_w_up, B_w_down):
    def _A_ref(x, wq, wk, wv, wo):
        B, S, D = x.shape
        H = 32
        Dh = D // H
        q = (lambda x, *d: x.permute(*d))((x @ wq).reshape(B, S, H, Dh), 0, 2, 1, 3)
        k = x @ wk
        v = x @ wv
        scores = torch.einsum("bhqd,bkd->bhqk", q, k) * (Dh ** -0.5)
        p = _softmax(scores, axis=-1)
        o = torch.einsum("bhqk,bkd->bhqd", p, v)
        return (lambda x, *d: x.permute(*d))(o, 0, 2, 1, 3).reshape(B, S, H * Dh) @ wo

    def _B_ref(x, w_gate, w_up, w_down):
        g = _silu(x @ w_gate)
        u = x @ w_up
        return (g * u) @ w_down

    x = A_x
    xn = _layernorm(x, axis=-1)
    return x + _A_ref(xn, A_wq, A_wk, A_wv, A_wo) + _B_ref(xn, B_w_gate, B_w_up, B_w_down)
```

```cpp
#include <hip/hip_runtime.h>
#include <hip/hip_bf16.h>
#include <stdint.h>

typedef __attribute__((ext_vector_type(8))) __bf16 bf16x8;
typedef __attribute__((ext_vector_type(2))) __bf16 bf16x2;
typedef __attribute__((ext_vector_type(4))) short s16x4;
typedef __attribute__((ext_vector_type(16))) float f32x16;
typedef __attribute__((ext_vector_type(4))) float f32x4;
typedef __attribute__((ext_vector_type(2))) float f32x2;
typedef __attribute__((ext_vector_type(4))) unsigned u32x4;
typedef __attribute__((ext_vector_type(2))) unsigned u32x2;
#define LAS __attribute__((address_space(3)))
typedef unsigned short u16;

constexpr int S_ = 2048, D_ = 640, F_ = 1728;
constexpr int KS = 74;
constexpr size_t OFF_XN = 0;
constexpr size_t OFF_Q = OFF_XN + 2621440;
constexpr size_t OFF_KV = OFF_Q + 2621440;
constexpr size_t OFF_A = OFF_KV + 196608;
constexpr size_t OFF_W = OFF_A + 9699328;

__device__ __forceinline__ void glds16(const void* g, unsigned lds) {
  unsigned keep;
  asm volatile("s_mov_b32 %0, m0\n\ts_mov_b32 m0, %2\n\ts_nop 0\n\tglobal_load_lds_dwordx4 %1, off\n\ts_mov_b32 m0, %0"
               : "=&s"(keep) : "v"(g), "s"(lds) : "memory");
}
__device__ __forceinline__ unsigned pk2(float a, float b) {
  bf16x2 v = {(__bf16)a, (__bf16)b};
  return __builtin_bit_cast(unsigned, v);
}
__device__ __forceinline__ float bfr(float a) { return (float)(__bf16)a; }
__device__ __forceinline__ float lo16(unsigned u) { return __uint_as_float(u << 16); }
__device__ __forceinline__ float hi16(unsigned u) { return __uint_as_float(u & 0xffff0000u); }
__device__ __forceinline__ bf16x8 ldsr128(unsigned a) { return *(const LAS bf16x8*)(uintptr_t)a; }
__device__ __forceinline__ u32x2 ldsr64(unsigned a) { return *(const LAS u32x2*)(uintptr_t)a; }
__device__ __forceinline__ bf16x8 ldstr(unsigned a0, unsigned a1) {
  s16x4 x = __builtin_amdgcn_ds_read_tr16_b64_v4i16((LAS s16x4*)(uintptr_t)a0);
  s16x4 y = __builtin_amdgcn_ds_read_tr16_b64_v4i16((LAS s16x4*)(uintptr_t)a1);
  u32x2 xa = __builtin_bit_cast(u32x2, x), ya = __builtin_bit_cast(u32x2, y);
  u32x4 r = {xa[0], xa[1], ya[0], ya[1]};
  return __builtin_bit_cast(bf16x8, r);
}
__device__ __forceinline__ f32x16 mma32(bf16x8 a, bf16x8 b, f32x16 c) { return __builtin_amdgcn_mfma_f32_32x32x16_bf16(a, b, c, 0, 0, 0); }
__device__ __forceinline__ f32x4 mma16(bf16x8 a, bf16x8 b, f32x4 c) { return __builtin_amdgcn_mfma_f32_16x16x32_bf16(a, b, c, 0, 0, 0); }
__device__ __forceinline__ bf16x8 asbf(u32x4 v) { return __builtin_bit_cast(bf16x8, v); }
__device__ __forceinline__ float xhalf_max(float v) {
  auto r = __builtin_amdgcn_permlane32_swap(__float_as_uint(v), __float_as_uint(v), false, false);
  return fmaxf(__uint_as_float(r[0]), __uint_as_float(r[1]));
}
#define WAITV(n) asm volatile("s_waitcnt vmcnt(" #n ")" ::: "memory")
#define BAR() asm volatile("s_waitcnt lgkmcnt(0)\n\ts_barrier" ::: "memory")

constexpr int K1_RING = 40960, K1_KV = K1_RING + 5 * 16384, K1_LDS = K1_KV + 25600 + 128;
__global__ void __launch_bounds__(512, 2) k1_ln_qkv(const u16* __restrict__ x, const u16* __restrict__ wq, const u16* __restrict__ wk,
                                                    const u16* __restrict__ wv, char* __restrict__ ws) {
  __shared__ __attribute__((aligned(16))) char smem[K1_LDS];
  const unsigned sb = (unsigned)(uintptr_t)(LAS char*)smem;
  const int tid = threadIdx.x, l = tid & 63, w = __builtin_amdgcn_readfirstlane(tid >> 6);
  const int b = blockIdx.x, cg = (b >> 3) & 3, rb = (b & 7) + 8 * (b >> 5);
  const int r = tid >> 4, p = tid & 15;
  u32x4 xv[5];
  const u16* xp = x + (size_t)(rb * 32 + r) * D_ + p * 8;
#pragma unroll
  for (int i = 0; i < 5; i++) asm volatile("global_load_dwordx4 %0, %1, off offset:%2" : "=v"(xv[i]) : "v"(xp), "i"(i * 256));
  const int n0 = cg * 160 + w * 32;
  const u16* wsrc = wq + (size_t)(l >> 2) * D_ + n0 + (l & 3) * 8;
  const unsigned ring = sb + K1_RING + w * 16384;
  const int role = w < 5 ? 0 : ((w == 5 && cg < 2) ? 1 : 2);
  if (role == 0) {
#pragma unroll
    for (int j = 0; j < 16; j++) glds16(wsrc + (size_t)j * 16 * D_, ring + j * 1024);
  } else if (role == 1) {
    const u16* src = (cg == 0 ? wk : wv) + l * 8;
#pragma unroll
    for (int j = 0; j < 25; j++) glds16(src + j * 512, sb + K1_KV + j * 1024);
  }
  asm volatile("s_cmp_lg_u32 %5, 0\n\ts_cbranch_scc1 1f\n\ts_waitcnt vmcnt(16)\n\ts_branch 3f\n"
               "1:\n\ts_cmp_lg_u32 %5, 1\n\ts_cbranch_scc1 2f\n\ts_waitcnt vmcnt(25)\n\ts_branch 3f\n"
               "2:\n\ts_waitcnt vmcnt(0)\n3:"
               : "+v"(xv[0]), "+v"(xv[1]), "+v"(xv[2]), "+v"(xv[3]), "+v"(xv[4]) : "s"(role) : "memory", "scc");
  float v[40];
#pragma unroll
  for (int i = 0; i < 5; i++)
#pragma unroll
    for (int j = 0; j < 4; j++) { v[i * 8 + 2 * j] = lo16(xv[i][j]); v[i * 8 + 2 * j + 1] = hi16(xv[i][j]); }
  float s = 0.f;
#pragma unroll
  for (int i = 0; i < 40; i++) s += v[i];
  s += __shfl_xor(s, 1); s += __shfl_xor(s, 2); s += __shfl_xor(s, 4); s += __shfl_xor(s, 8);
  const float mean = s * (1.f / D_);
  float q2 = 0.f;
#pragma unroll
  for (int i = 0; i < 40; i++) { float d = v[i] - mean; q2 += d * d; }
  q2 += __shfl_xor(q2, 1); q2 += __shfl_xor(q2, 2); q2 += __shfl_xor(q2, 4); q2 += __shfl_xor(q2, 8);
  const float mb = bfr(mean), vb = bfr(q2 * (1.f / D_));
  const float sd = bfr(__builtin_sqrtf(bfr(vb + 1e-5f)));
  const float rs = 1.f / sd;
  char* xnf = ws + OFF_XN + (size_t)rb * 40960;
#pragma unroll
  for (int i = 0; i < 5; i++) {
    u32x4 o;
#pragma unroll
    for (int j = 0; j < 4; j++) {
      unsigned d = pk2(v[i * 8 + 2 * j] - mb, v[i * 8 + 2 * j + 1] - mb);
      o[j] = pk2(lo16(d) * rs, hi16(d) * rs);
    }
    const int c = p + 16 * i;
    *(LAS u32x4*)(uintptr_t)(sb + c * 512 + ((r ^ (c & 7)) << 4)) = o;
    if (i == 4 ? ((p & 3) == cg) : (i == cg)) *(u32x4*)(xnf + c * 512 + r * 16) = o;
  }
  BAR();
  const int hh = l >> 5, m = l & 31;
  const int trow = hh * 8 + ((l & 15) >> 2), tcol = ((l >> 4) & 1) * 32 + (l & 3) * 8;
  if (w < 5) {
    f32x16 acc = {};
    const unsigned ta = ring + trow * 64 + tcol;
#pragma unroll
    for (int st = 0; st < 10; st++) {
      if (st <= 6) WAITV(12); else if (st == 7) WAITV(8); else if (st == 8) WAITV(4); else WAITV(0);
#pragma unroll
      for (int k = 0; k < 4; k++) {
        const int c = (st * 4 + k) * 2 + hh;
        bf16x8 xf = ldsr128(sb + c * 512 + ((m ^ (c & 7)) << 4));
        bf16x8 wf = ldstr(ta + (st & 3) * 4096 + k * 1024, ta + (st & 3) * 4096 + k * 1024 + 256);
        acc = mma32(wf, xf, acc);
      }
      if (st + 4 < 10) {
        asm volatile("s_waitcnt lgkmcnt(0)" ::: "memory");
#pragma unroll
        for (int j = 0; j < 4; j++) glds16(wsrc + (size_t)((st + 4) * 64 + j * 16) * D_, ring + (st & 3) * 4096 + j * 1024);
      }
    }
    const float sc = 0.22360679774997896f * 1.4426950408889634f;
    u16* qp = (u16*)(ws + OFF_Q) + (size_t)(rb * 32 + m) * D_ + n0 + 4 * hh;
#pragma unroll
    for (int g = 0; g < 4; g++) {
      u32x2 o = {pk2(bfr(acc[4 * g]) * sc, bfr(acc[4 * g + 1]) * sc), pk2(bfr(acc[4 * g + 2]) * sc, bfr(acc[4 * g + 3]) * sc)};
      *(u32x2*)(qp + 8 * g) = o;
    }
  } else if (w == 5 && cg < 2) {
    WAITV(0);
    f32x16 acc = {};
    const unsigned ta = sb + K1_KV + trow * 40 + tcol;
    char* tile = ws + OFF_KV + (size_t)rb * 3072;
    if (cg == 0) {
#pragma unroll
      for (int k = 0; k < 40; k++) {
        const int c = k * 2 + hh;
        acc = mma32(ldstr(ta + k * 640, ta + k * 640 + 160), ldsr128(sb + c * 512 + ((m ^ (c & 7)) << 4)), acc);
      }
      u32x4 o = {pk2(acc[0], acc[1]), pk2(acc[2], acc[3]), pk2(acc[4], acc[5]), pk2(acc[6], acc[7])};
      *(u32x4*)(tile + l * 16) = o;
      u32x2 o2 = {hh ? 0x3f803f80u : pk2(acc[8], acc[9]), hh ? 0u : pk2(acc[10], acc[11])};
      *(u32x2*)(tile + 1024 + l * 8) = o2;
    } else {
#pragma unroll
      for (int k = 0; k < 40; k++) {
        const int c = k * 2 + hh;
        acc = mma32(ldsr128(sb + c * 512 + ((m ^ (c & 7)) << 4)), ldstr(ta + k * 640, ta + k * 640 + 160), acc);
      }
      if (m < 24) {
#pragma unroll
        for (int s2 = 0; s2 < 2; s2++) {
          u32x4 o = {pk2(acc[8 * s2], acc[8 * s2 + 1]), pk2(acc[8 * s2 + 2], acc[8 * s2 + 3]), pk2(acc[8 * s2 + 4], acc[8 * s2 + 5]), pk2(acc[8 * s2 + 6], acc[8 * s2 + 7])};
          if (m >= 20) o = u32x4{0x3f803f80u, 0x3f803f80u, 0x3f803f80u, 0x3f803f80u};
          *(u32x4*)(tile + 1536 + (s2 * 2 + hh) * 384 + m * 16) = o;
        }
      }
    }
  }
}

constexpr int K2_LDS = 3 * 24576;
constexpr int N_ATT = 256, N_GU = 216, N_PACK = 40;

__device__ __forceinline__ void packP(const f32x16& s, bf16x8& p0, bf16x8& p1) {
  u32x4 a, b;
#pragma unroll
  for (int j = 0; j < 4; j++) {
    a[j] = pk2(__builtin_amdgcn_exp2f(s[2 * j]), __builtin_amdgcn_exp2f(s[2 * j + 1]));
    b[j] = pk2(__builtin_amdgcn_exp2f(s[8 + 2 * j]), __builtin_amdgcn_exp2f(s[8 + 2 * j + 1]));
  }
  p0 = asbf(a); p1 = asbf(b);
}
__device__ __forceinline__ float max16(const f32x16& s) {
  float a = fmaxf(fmaxf(s[0], s[1]), fmaxf(s[2], s[3]));
#pragma unroll
  for (int j = 4; j < 16; j += 2) a = fmaxf(a, fmaxf(s[j], s[j + 1]));
  return a;
}
__device__ __forceinline__ float split_ref(float mv, unsigned& slot) {
  float hi = bfr(mv), lo = bfr(mv - hi);
  slot = pk2(-hi, -lo);
  return hi + lo;
}

__device__ __forceinline__ void attn_body(int a, char* __restrict__ ws, unsigned sb) {
  const int tid = threadIdx.x, l = tid & 63, w = __builtin_amdgcn_readfirstlane(tid >> 6);
  const int hh = l >> 5, m = l & 31;
  const int qb = a & 63, head = (a >> 6) * 8 + w;
  const char* kvg = ws + OFF_KV;
  const char* src = kvg + w * 3072 + l * 16;
#pragma unroll
  for (int c = 0; c < 2; c++)
#pragma unroll
    for (int j = 0; j < 3; j++) glds16(src + c * 24576 + j * 1024, sb + c * 24576 + w * 3072 + j * 1024);
  const int row = qb * 32 + m;
  const u16* qp = (const u16*)(ws + OFF_Q) + (size_t)row * D_ + head * 20;
  u32x2 qa = *(const u32x2*)(qp + 4 * hh), qc = *(const u32x2*)(qp + 8 + 4 * hh), qd = *(const u32x2*)(qp + 16);
  const bf16x8 q1 = asbf(u32x4{qa[0], qa[1], qc[0], qc[1]});
  u32x4 q2 = {hh ? 0u : qd[0], hh ? 0u : qd[1], 0u, 0u};
  const unsigned ka = l * 16, kb = 1024 + l * 8, va = 1536 + hh * 384 + (m < 20 ? m : 20) * 16;
  WAITV(3); BAR();
  auto qk = [&](unsigned tb) {
    bf16x8 k1 = ldsr128(tb + ka);
    u32x2 k2 = ldsr64(tb + kb);
    f32x16 z = {};
    z = mma32(k1, q1, z);
    return mma32(asbf(u32x4{k2[0], k2[1], 0u, 0u}), asbf(q2), z);
  };
  f32x16 s = qk(sb), o = {};
  unsigned slot;
  float mref = split_ref(xhalf_max(max16(s)), slot);
  if (hh) q2[0] = slot;
#pragma unroll
  for (int j = 0; j < 16; j++) s[j] -= mref;
  unsigned cur = sb, nxt = sb + 24576, fre = sb + 49152;
  for (int c = 0; c < 8; c++) {
#pragma unroll
    for (int t = 0; t < 8; t++) {
      f32x16 sn;
      if (t == 7) {
        WAITV(0); BAR();
        if (c < 6) {
#pragma unroll
          for (int j = 0; j < 3; j++) glds16(src + (c + 2) * 24576 + j * 1024, __builtin_amdgcn_readfirstlane(fre) + w * 3072 + j * 1024);
        }
        sn = qk(nxt);
      } else {
        sn = qk(cur + (t + 1) * 3072);
      }
      if ((t & 3) == 3) {
        if (__builtin_expect(__any(o[12] > 1073741824.f), 0)) {
          float lm = xhalf_max(o[12]);
          int e = __builtin_amdgcn_frexp_expf(lm) - 1;
          float mn = split_ref(mref + (float)(e > 0 ? e : 0), slot);
          float dl = mn - mref, f = __builtin_amdgcn_exp2f(-dl);
          mref = mn;
          if (hh) q2[0] = slot;
#pragma unroll
          for (int j = 0; j < 16; j++) { o[j] *= f; s[j] -= dl; sn[j] -= dl; }
        }
      }
      bf16x8 v0 = ldsr128(cur + t * 3072 + va), v1 = ldsr128(cur + t * 3072 + va + 768);
      bf16x8 p0, p1;
      packP(s, p0, p1);
      o = mma32(v0, p0, o);
      o = mma32(v1, p1, o);
      s = sn;
    }
    unsigned tmp = cur; cur = nxt; nxt = fre; fre = tmp;
  }
  float chk = o[12];
#pragma unroll
  for (int j = 0; j < 12; j++) chk = fmaxf(chk, fabsf((hh && j >= 8) ? 0.f : o[j]) );
  bool bad = !(chk < 1e37f);
#pragma unroll
  for (int j = 0; j < 13; j++) bad = bad || ((hh && j >= 8 && j < 12) ? false : (o[j] != o[j]));
  if (__builtin_expect(__any(bad), 0)) {
    if (hh) q2[0] = 0u;
    float mx = -INFINITY;
    o = f32x16{};
    for (int t = 0; t < 64; t++) {
      const char* tb = kvg + t * 3072;
      u32x2 k2 = *(const u32x2*)(tb + kb);
      f32x16 z = {};
      z = mma32(*(const bf16x8*)(tb + ka), q1, z);
      z = mma32(asbf(u32x4{k2[0], k2[1], 0u, 0u}), asbf(q2), z);
      float mn = fmaxf(mx, xhalf_max(max16(z)));
      float f = __builtin_amdgcn_exp2f(mx - mn);
      mx = mn;
#pragma unroll
      for (int j = 0; j < 16; j++) { o[j] *= f; z[j] -= mn; }
      bf16x8 p0, p1;
      packP(z, p0, p1);
      o = mma32(*(const bf16x8*)(tb + va), p0, o);
      o = mma32(*(const bf16x8*)(tb + va + 768), p1, o);
    }
  }
  const float inv = 1.f / o[12];
  char* ab = ws + OFF_A + (size_t)(row >> 4) * (KS * 1024) + (row & 15) * 16;
#pragma unroll
  for (int g = 0; g < 3; g++) {
    if (g < 2 || !hh) {
      const int k = head * 20 + 8 * g + 4 * hh;
      u32x2 ov = {pk2(o[4 * g] * inv, o[4 * g + 1] * inv), pk2(o[4 * g + 2] * inv, o[4 * g + 3] * inv)};
      *(u32x2*)(ab + (k >> 3) * 256 + ((k >> 2) & 1) * 8) = ov;
    }
  }
}

__device__ __forceinline__ void gu_body(int id, const u16* __restrict__ wg, const u16* __restrict__ wu, char* __restrict__ ws, unsigned sb) {
  const int tid = threadIdx.x, l = tid & 63, w = __builtin_amdgcn_readfirstlane(tid >> 6);
  const int hh = l >> 5, m = l & 31;
  const int T = (id & 7) * 27 + (id >> 3), nt = T >> 3, mt = T & 7;
  const char* xs = ws + OFF_XN + (size_t)(mt * 8 + w) * 40960 + l * 16;
  const u16* wsrc = ((w & 1) ? wu : wg) + (size_t)((w >> 1) * 8 + ((l & 31) >> 2)) * F_ + nt * 64 + (l >> 5) * 32 + (l & 3) * 8;
  auto issue = [&](int st, unsigned buf) {
    glds16(xs + st * 2048, buf + w * 2048);
    glds16(xs + st * 2048 + 1024, buf + w * 2048 + 1024);
    glds16(wsrc + (size_t)st * 32 * F_, buf + 16384 + w * 1024);
  };
  issue(0, sb); issue(1, sb + 24576);
  const int wm = w & 3, wn = w >> 2;
  const unsigned xa = wm * 4096 + l * 16;
  const unsigned wa = 16384 + hh * 2048 + wn * 512 + ((l & 15) >> 2) * 64 + ((l >> 4) & 1) * 32 + (l & 3) * 8;
  f32x16 acc[2][2] = {};
  unsigned cur = sb, nxt = sb + 24576, fre = sb + 49152;
  for (int st = 0; st < 20; st++) {
    if (st < 19) WAITV(3); else WAITV(0);
    BAR();
    if (st < 18) issue(st + 2, __builtin_amdgcn_readfirstlane(fre));
#pragma unroll
    for (int s = 0; s < 2; s++) {
      bf16x8 x0 = ldsr128(cur + xa + s * 1024), x1 = ldsr128(cur + xa + 2048 + s * 1024);
      bf16x8 g = ldstr(cur + wa + s * 4096, cur + wa + s * 4096 + 256);
      bf16x8 u = ldstr(cur + wa + s * 4096 + 1024, cur + wa + s * 4096 + 1024 + 256);
      acc[0][0] = mma32(g, x0, acc[0][0]); acc[0][1] = mma32(g, x1, acc[0][1]);
      acc[1][0] = mma32(u, x0, acc[1][0]); acc[1][1] = mma32(u, x1, acc[1][1]);
    }
    unsigned tmp = cur; cur = nxt; nxt = fre; fre = tmp;
  }
#pragma unroll
  for (int mb = 0; mb < 2; mb++) {
    const int row = mt * 256 + wm * 64 + mb * 32 + m;
    char* ab = ws + OFF_A + (size_t)(row >> 4) * (KS * 1024) + (20 + nt * 2 + wn) * 1024 + (row & 15) * 16 + hh * 8;
#pragma unroll
    for (int g = 0; g < 4; g++) {
      float h[4];
#pragma unroll
      for (int i = 0; i < 4; i++) {
        float gv = acc[0][mb][4 * g + i], uv = acc[1][mb][4 * g + i];
        h[i] = gv * __builtin_amdgcn_rcpf(1.f + __builtin_amdgcn_exp2f(gv * -1.4426950408889634f)) * uv;
      }
      *(u32x2*)(ab + g * 256) = u32x2{pk2(h[0], h[1]), pk2(h[2], h[3])};
    }
  }
}

__device__ __forceinline__ void pack_body(int id, const u16* __restrict__ wo, const u16* __restrict__ wd, char* __restrict__ ws) {
  for (int u = id * 512 + threadIdx.x; u < 296 * 80; u += N_PACK * 512) {
    const int ko = u / 80, no = u - ko * 80;
    const u16* src = (ko < 80 ? wo + (size_t)ko * 8 * D_ : wd + (size_t)(ko - 80) * 8 * D_) + no * 8;
    u32x4 r[8];
#pragma unroll
    for (int i = 0; i < 8; i++) r[i] = *(const u32x4*)(src + i * D_);
    char* dst = ws + OFF_W + (size_t)(no >> 1) * (KS * 1024) + ko * 256 + (no & 1) * 128;
#pragma unroll
    for (int j = 0; j < 4; j++) {
      u32x4 e, o;
#pragma unroll
      for (int i = 0; i < 4; i++) {
        e[i] = __builtin_amdgcn_perm(r[2 * i + 1][j], r[2 * i][j], 0x05040100u);
        o[i] = __builtin_amdgcn_perm(r[2 * i + 1][j], r[2 * i][j], 0x07060302u);
      }
      *(u32x4*)(dst + (2 * j) * 16) = e;
      *(u32x4*)(dst + (2 * j + 1) * 16) = o;
    }
  }
}

__global__ void __launch_bounds__(512, 4) k2_mix(const u16* __restrict__ wg, const u16* __restrict__ wu, const u16* __restrict__ wo,
                                                 const u16* __restrict__ wd, char* __restrict__ ws) {
  __shared__ __attribute__((aligned(16))) char smem[K2_LDS];
  const unsigned sb = (unsigned)(uintptr_t)(LAS char*)smem;
  const int b = blockIdx.x;
  if (b < N_ATT) attn_body(b, ws, sb);
  else if (b < N_ATT + N_GU) gu_body(b - N_ATT, wg, wu, ws, sb);
  else pack_body(b - N_ATT - N_GU, wo, wd, ws);
}

constexpr int K3_DEPTH = 5;
__global__ void __launch_bounds__(256, 1) k3_out(const u16* __restrict__ x, const char* __restrict__ ws, u16* __restrict__ out) {
  __shared__ __attribute__((aligned(16))) float red[4 * 64 * 84];
  const int tid = threadIdx.x, l = tid & 63, w = __builtin_amdgcn_readfirstlane(tid >> 6);
  const int b = blockIdx.x, xcd = b & 7, j = b >> 3;
  const int mt = (xcd >> 1) * 8 + (j & 7), nt = (xcd & 1) * 4 + (j >> 3);
  const char* ap = ws + OFF_A + (size_t)mt * 4 * (KS * 1024) + l * 16;
  const char* bp = ws + OFF_W + (size_t)nt * 5 * (KS * 1024) + l * 16;
  bf16x8 fa[K3_DEPTH][4], fb[K3_DEPTH][5];
  f32x4 acc[4][5] = {};
  auto load = [&](int i, int slot) {
    int ks = w + 4 * i; ks = ks < KS ? ks : KS - 1;
#pragma unroll
    for (int t = 0; t < 5; t++) fb[slot][t] = *(const bf16x8*)(bp + (size_t)(t * KS + ks) * 1024);
#pragma unroll
    for (int t = 0; t < 4; t++) fa[slot][t] = *(const bf16x8*)(ap + (size_t)(t * KS + ks) * 1024);
  };
#pragma unroll
  for (int i = 0; i < K3_DEPTH; i++) load(i, i);
  const int er = tid >> 2, ec = (tid & 3) * 20;
  const size_t eo = (size_t)(mt * 64 + er) * D_ + nt * 80 + ec;
  u32x2 xr[5];
#pragma unroll
  for (int i = 0; i < 5; i++) xr[i] = *(const u32x2*)(x + eo + 4 * i);
#pragma unroll
  for (int i = 0; i < 19; i++) {
    const int slot = i % K3_DEPTH;
    if (i < 18 || w < 2) {
#pragma unroll
      for (int a = 0; a < 4; a++)
#pragma unroll
        for (int t = 0; t < 5; t++) acc[a][t] = mma16(fb[slot][t], fa[slot][a], acc[a][t]);
    }
    if (i + K3_DEPTH < 19) load(i + K3_DEPTH, slot);
  }
  float* rw = red + w * (64 * 84);
#pragma unroll
  for (int a = 0; a < 4; a++)
#pragma unroll
    for (int t = 0; t < 5; t++) *(f32x4*)(rw + (a * 16 + (l & 15)) * 84 + t * 16 + (l >> 4) * 4) = acc[a][t];
  __syncthreads();
#pragma unroll
  for (int i = 0; i < 5; i++) {
    const float* rp = red + er * 84 + ec + 4 * i;
    f32x4 v = *(const f32x4*)rp + *(const f32x4*)(rp + 64 * 84) + *(const f32x4*)(rp + 2 * 64 * 84) + *(const f32x4*)(rp + 3 * 64 * 84);
    u32x2 o = {pk2(v[0] + lo16(xr[i][0]), v[1] + hi16(xr[i][0])), pk2(v[2] + lo16(xr[i][1]), v[3] + hi16(xr[i][1]))};
    *(u32x2*)(out + eo + 4 * i) = o;
  }
}

extern "C" void kernel_launch(void* const* d_in, const int* in_sizes, int n_in, void* d_out, int out_size, void* d_ws, size_t ws_size,
                              hipStream_t stream) {
  const u16* x = (const u16*)d_in[0];
  char* ws = (char*)d_ws;
  k1_ln_qkv<<<256, 512, 0, stream>>>(x, (const u16*)d_in[1], (const u16*)d_in[2], (const u16*)d_in[3], ws);
  k2_mix<<<N_ATT + N_GU + N_PACK, 512, 0, stream>>>((const u16*)d_in[6], (const u16*)d_in[7], (const u16*)d_in[4], (const u16*)d_in[8], ws);
  k3_out<<<256, 256, 0, stream>>>(x, ws, (u16*)d_out);
}
```

```cpp
#include <hip/hip_runtime.h>
#include <hip/hip_bf16.h>
#include <stdint.h>

typedef __attribute__((ext_vector_type(8))) __bf16 bf16x8;
typedef __attribute__((ext_vector_type(2))) __bf16 bf16x2;
typedef __attribute__((ext_vector_type(4))) short s16x4;
typedef __attribute__((ext_vector_type(16))) float f32x16;
typedef __attribute__((ext_vector_type(4))) float f32x4;
typedef __attribute__((ext_vector_type(2))) float f32x2;
typedef __attribute__((ext_vector_type(4))) unsigned u32x4;
typedef __attribute__((ext_vector_type(2))) unsigned u32x2;
#define LAS __attribute__((address_space(3)))
typedef unsigned short u16;

constexpr int S_ = 2048, D_ = 640, F_ = 1728;
constexpr int KS = 74;
constexpr size_t OFF_XN = 0;
constexpr size_t OFF_Q = OFF_XN + 2621440;
constexpr size_t OFF_KV = OFF_Q + 2621440;
constexpr size_t OFF_A = OFF_KV + 196608;
constexpr size_t OFF_W = OFF_A + 9699328;

__device__ __forceinline__ void glds16(const void* g, unsigned lds) {
  unsigned keep;
  asm volatile("s_mov_b32 %0, m0\n\ts_mov_b32 m0, %2\n\ts_nop 0\n\tglobal_load_lds_dwordx4 %1, off\n\ts_mov_b32 m0, %0"
               : "=&s"(keep) : "v"(g), "s"(lds) : "memory");
}
__device__ __forceinline__ unsigned pk2(float a, float b) {
  bf16x2 v = {(__bf16)a, (__bf16)b};
  return __builtin_bit_cast(unsigned, v);
}
__device__ __forceinline__ float bfr(float a) { return (float)(__bf16)a; }
__device__ __forceinline__ float lo16(unsigned u) { return __uint_as_float(u << 16); }
__device__ __forceinline__ float hi16(unsigned u) { return __uint_as_float(u & 0xffff0000u); }
__device__ __forceinline__ bf16x8 ldsr128(unsigned a) { return *(const LAS bf16x8*)(uintptr_t)a; }
__device__ __forceinline__ u32x2 ldsr64(unsigned a) { return *(const LAS u32x2*)(uintptr_t)a; }
__device__ __forceinline__ bf16x8 ldstr(unsigned a0, unsigned a1) {
  s16x4 x = __builtin_amdgcn_ds_read_tr16_b64_v4i16((LAS s16x4*)(uintptr_t)a0);
  s16x4 y = __builtin_amdgcn_ds_read_tr16_b64_v4i16((LAS s16x4*)(uintptr_t)a1);
  u32x2 xa = __builtin_bit_cast(u32x2, x), ya = __builtin_bit_cast(u32x2, y);
  u32x4 r = {xa[0], xa[1], ya[0], ya[1]};
  return __builtin_bit_cast(bf16x8, r);
}
__device__ __forceinline__ f32x16 mma32(bf16x8 a, bf16x8 b, f32x16 c) { return __builtin_amdgcn_mfma_f32_32x32x16_bf16(a, b, c, 0, 0, 0); }
__device__ __forceinline__ f32x4 mma16(bf16x8 a, bf16x8 b, f32x4 c) { return __builtin_amdgcn_mfma_f32_16x16x32_bf16(a, b, c, 0, 0, 0); }
__device__ __forceinline__ bf16x8 asbf(u32x4 v) { return __builtin_bit_cast(bf16x8, v); }
__device__ __forceinline__ float xhalf_max(float v) {
  auto r = __builtin_amdgcn_permlane32_swap(__float_as_uint(v), __float_as_uint(v), false, false);
  return fmaxf(__uint_as_float(r[0]), __uint_as_float(r[1]));
}
#define WAITV(n) asm volatile("s_waitcnt vmcnt(" #n ")" ::: "memory")
#define BAR() asm volatile("s_waitcnt lgkmcnt(0)\n\ts_barrier" ::: "memory")

constexpr int K1_RING = 40960, K1_KV = K1_RING + 5 * 16384, K1_LDS = K1_KV + 25600 + 128;
__global__ void __launch_bounds__(512, 2) k1_ln_qkv(const u16* __restrict__ x, const u16* __restrict__ wq, const u16* __restrict__ wk,
                                                    const u16* __restrict__ wv, char* __restrict__ ws) {
  __shared__ __attribute__((aligned(16))) char smem[K1_LDS];
  const unsigned sb = (unsigned)(uintptr_t)(LAS char*)smem;
  const int tid = threadIdx.x, l = tid & 63, w = __builtin_amdgcn_readfirstlane(tid >> 6);
  const int b = blockIdx.x, cg = (b >> 3) & 3, rb = (b & 7) + 8 * (b >> 5);
  const int r = tid >> 4, p = tid & 15;
#define STAMP(i)
  u32x4 xv[5];
  const u16* xp = x + (size_t)(rb * 32 + r) * D_ + p * 8;
#pragma unroll
  for (int i = 0; i < 5; i++) asm volatile("global_load_dwordx4 %0, %1, off offset:%2" : "=v"(xv[i]) : "v"(xp), "i"(i * 256));
  const int n0 = cg * 160 + w * 32;
  const u16* wsrc = wq + (size_t)(l >> 2) * D_ + n0 + (l & 3) * 8;
  const unsigned ring = sb + K1_RING + w * 16384;
  const int role = w < 5 ? 0 : ((w == 5 && cg < 2) ? 1 : 2);
  if (role == 0) {
#pragma unroll
    for (int j = 0; j < 16; j++) glds16(wsrc + (size_t)j * 16 * D_, ring + j * 1024);
  } else if (role == 1) {
    const u16* src = (cg == 0 ? wk : wv) + l * 8;
#pragma unroll
    for (int j = 0; j < 25; j++) glds16(src + j * 512, sb + K1_KV + j * 1024);
  }
  asm volatile("s_cmp_lg_u32 %5, 0\n\ts_cbranch_scc1 1f\n\ts_waitcnt vmcnt(16)\n\ts_branch 3f\n"
               "1:\n\ts_cmp_lg_u32 %5, 1\n\ts_cbranch_scc1 2f\n\ts_waitcnt vmcnt(25)\n\ts_branch 3f\n"
               "2:\n\ts_waitcnt vmcnt(0)\n3:"
               : "+v"(xv[0]), "+v"(xv[1]), "+v"(xv[2]), "+v"(xv[3]), "+v"(xv[4]) : "s"(role) : "memory", "scc");
  STAMP(1)
  float v[40];
#pragma unroll
  for (int i = 0; i < 5; i++)
#pragma unroll
    for (int j = 0; j < 4; j++) { v[i * 8 + 2 * j] = lo16(xv[i][j]); v[i * 8 + 2 * j + 1] = hi16(xv[i][j]); }
  float s = 0.f;
#pragma unroll
  for (int i = 0; i < 40; i++) s += v[i];
  s += __shfl_xor(s, 1); s += __shfl_xor(s, 2); s += __shfl_xor(s, 4); s += __shfl_xor(s, 8);
  const float mean = s * (1.f / D_);
  float q2 = 0.f;
#pragma unroll
  for (int i = 0; i < 40; i++) { float d = v[i] - mean; q2 += d * d; }
  q2 += __shfl_xor(q2, 1); q2 += __shfl_xor(q2, 2); q2 += __shfl_xor(q2, 4); q2 += __shfl_xor(q2, 8);
  const float mb = bfr(mean), vb = bfr(q2 * (1.f / D_));
  const float sd = bfr(__builtin_sqrtf(bfr(vb + 1e-5f)));
  const float rs = 1.f / sd;
  char* xnf = ws + OFF_XN + (size_t)rb * 40960;
#pragma unroll
  for (int i = 0; i < 5; i++) {
    u32x4 o;
#pragma unroll
    for (int j = 0; j < 4; j++) {
      unsigned d = pk2(v[i * 8 + 2 * j] - mb, v[i * 8 + 2 * j + 1] - mb);
      o[j] = pk2(lo16(d) * rs, hi16(d) * rs);
    }
    const int c = p + 16 * i;
    *(LAS u32x4*)(uintptr_t)(sb + c * 512 + ((r ^ (c & 7)) << 4)) = o;
    if (i == 4 ? ((p & 3) == cg) : (i == cg)) *(u32x4*)(xnf + c * 512 + r * 16) = o;
  }
  STAMP(2)
  BAR();
  STAMP(3)
  const int hh = l >> 5, m = l & 31;
  const int trow = hh * 8 + ((l & 15) >> 2), tcol = ((l >> 4) & 1) * 32 + (l & 3) * 8;
  if (w < 5) {
    f32x16 acc = {};
    const unsigned ta = ring + trow * 64 + tcol;
#pragma unroll
    for (int st = 0; st < 10; st++) {
      if (st <= 6) WAITV(12); else if (st == 7) WAITV(8); else if (st == 8) WAITV(4); else WAITV(0);
#pragma unroll
      for (int k = 0; k < 4; k++) {
        const int c = (st * 4 + k) * 2 + hh;
        bf16x8 xf = ldsr128(sb + c * 512 + ((m ^ (c & 7)) << 4));
        bf16x8 wf = ldstr(ta + (st & 3) * 4096 + k * 1024, ta + (st & 3) * 4096 + k * 1024 + 256);
        acc = mma32(wf, xf, acc);
      }
      if (st + 4 < 10) {
        asm volatile("s_waitcnt lgkmcnt(0)" ::: "memory");
#pragma unroll
        for (int j = 0; j < 4; j++) glds16(wsrc + (size_t)((st + 4) * 64 + j * 16) * D_, ring + (st & 3) * 4096 + j * 1024);
      }
    }
    STAMP(4)
    const float sc = 0.22360679774997896f * 1.4426950408889634f;
    u16* qp = (u16*)(ws + OFF_Q) + (size_t)(rb * 32 + m) * D_ + n0 + 4 * hh;
#pragma unroll
    for (int g = 0; g < 4; g++) {
      u32x2 o = {pk2(bfr(acc[4 * g]) * sc, bfr(acc[4 * g + 1]) * sc), pk2(bfr(acc[4 * g + 2]) * sc, bfr(acc[4 * g + 3]) * sc)};
      *(u32x2*)(qp + 8 * g) = o;
    }
  } else if (w == 5 && cg < 2) {
    WAITV(0);
    f32x16 acc = {};
    const unsigned ta = sb + K1_KV + trow * 40 + tcol;
    char* tile = ws + OFF_KV + (size_t)rb * 3072;
    if (cg == 0) {
#pragma unroll
      for (int k = 0; k < 40; k++) {
        const int c = k * 2 + hh;
        acc = mma32(ldstr(ta + k * 640, ta + k * 640 + 160), ldsr128(sb + c * 512 + ((m ^ (c & 7)) << 4)), acc);
      }
      u32x4 o = {pk2(acc[0], acc[1]), pk2(acc[2], acc[3]), pk2(acc[4], acc[5]), pk2(acc[6], acc[7])};
      *(u32x4*)(tile + l * 16) = o;
      u32x2 o2 = {hh ? 0x3f803f80u : pk2(acc[8], acc[9]), hh ? 0u : pk2(acc[10], acc[11])};
      *(u32x2*)(tile + 1024 + l * 8) = o2;
    } else {
#pragma unroll
      for (int k = 0; k < 40; k++) {
        const int c = k * 2 + hh;
        acc = mma32(ldsr128(sb + c * 512 + ((m ^ (c & 7)) << 4)), ldstr(ta + k * 640, ta + k * 640 + 160), acc);
      }
      if (m < 24) {
#pragma unroll
        for (int s2 = 0; s2 < 2; s2++) {
          u32x4 o = {pk2(acc[8 * s2], acc[8 * s2 + 1]), pk2(acc[8 * s2 + 2], acc[8 * s2 + 3]), pk2(acc[8 * s2 + 4], acc[8 * s2 + 5]), pk2(acc[8 * s2 + 6], acc[8 * s2 + 7])};
          if (m >= 20) o = u32x4{0x3f803f80u, 0x3f803f80u, 0x3f803f80u, 0x3f803f80u};
          *(u32x4*)(tile + 1536 + (s2 * 2 + hh) * 384 + m * 16) = o;
        }
      }
    }
  }
}

constexpr int K2_LDS = 3 * 24576;
constexpr int N_ATT = 256, N_GU = 216, N_PACK = 40;

__device__ __forceinline__ void packP(const f32x16& s, bf16x8& p0, bf16x8& p1) {
  u32x4 a, b;
#pragma unroll
  for (int j = 0; j < 4; j++) {
    a[j] = pk2(__builtin_amdgcn_exp2f(s[2 * j]), __builtin_amdgcn_exp2f(s[2 * j + 1]));
    b[j] = pk2(__builtin_amdgcn_exp2f(s[8 + 2 * j]), __builtin_amdgcn_exp2f(s[8 + 2 * j + 1]));
  }
  p0 = asbf(a); p1 = asbf(b);
}
__device__ __forceinline__ float max16(const f32x16& s) {
  float a = fmaxf(fmaxf(s[0], s[1]), fmaxf(s[2], s[3]));
#pragma unroll
  for (int j = 4; j < 16; j += 2) a = fmaxf(a, fmaxf(s[j], s[j + 1]));
  return a;
}
__device__ __forceinline__ float split_ref(float mv, unsigned& slot) {
  float hi = bfr(mv), lo = bfr(mv - hi);
  slot = pk2(-hi, -lo);
  return hi + lo;
}

__device__ __forceinline__ void attn_body(int a, char* __restrict__ ws, unsigned sb) {
  const int tid = threadIdx.x, l = tid & 63, w = __builtin_amdgcn_readfirstlane(tid >> 6);
  const int hh = l >> 5, m = l & 31;
  const int qb = a & 63, head = (a >> 6) * 8 + w;
  const char* kvg = ws + OFF_KV;
  const char* src = kvg + w * 3072 + l * 16;
#pragma unroll
  for (int c = 0; c < 2; c++)
#pragma unroll
    for (int j = 0; j < 3; j++) glds16(src + c * 24576 + j * 1024, sb + c * 24576 + w * 3072 + j * 1024);
  const int row = qb * 32 + m;
  const u16* qp = (const u16*)(ws + OFF_Q) + (size_t)row * D_ + head * 20;
  u32x2 qa = *(const u32x2*)(qp + 4 * hh), qc = *(const u32x2*)(qp + 8 + 4 * hh), qd = *(const u32x2*)(qp + 16);
  const bf16x8 q1 = asbf(u32x4{qa[0], qa[1], qc[0], qc[1]});
  u32x4 q2 = {hh ? 0u : qd[0], hh ? 0u : qd[1], 0u, 0u};
  const unsigned ka = l * 16, kb = 1024 + l * 8, va = 1536 + hh * 384 + (m < 20 ? m : 20) * 16;
  WAITV(3); BAR();
#define SB() __builtin_amdgcn_sched_barrier(0)
#define EX(v) __builtin_amdgcn_exp2f(v)
#define TR2(a, b) __builtin_amdgcn_perm(__float_as_uint(b), __float_as_uint(a), 0x07060302u)
  const f32x16 zero = {};
  u32x4 pb = {0u, 0u, 0u, 0u};
  float e0 = 0.f, e1 = 0.f, e2 = 0.f, e3 = 0.f;
  __builtin_amdgcn_s_setprio(2);
  bf16x8 k1 = ldsr128(sb + ka), v0 = ldsr128(sb + va), v1 = ldsr128(sb + va + 768);
  u32x4 k2 = {0u, 0u, 0u, 0u};
  { u32x2 t2 = ldsr64(sb + kb); k2[0] = t2[0]; k2[1] = t2[1]; }
  f32x16 s = mma32(asbf(k2), asbf(q2), mma32(k1, q1, zero)), o = {};
  k1 = ldsr128(sb + 3072 + ka);
  { u32x2 t2 = ldsr64(sb + 3072 + kb); k2[0] = t2[0]; k2[1] = t2[1]; }
  unsigned slot;
  float mref = split_ref(xhalf_max(max16(s)) + 60.f, slot);
  if (hh) q2[0] = slot;
#pragma unroll
  for (int j = 0; j < 16; j++) s[j] -= mref;
  unsigned cur = sb, nxt = sb + 24576, fre = sb + 49152;
  for (int c = 0; c < 8; c++) {
    const unsigned ck = cur + ka, ck2 = cur + kb, cv = cur + va, nk = nxt + ka, nk2 = nxt + kb, nv = nxt + va;
#pragma unroll
    for (int t = 0; t < 8; t++) {
      if (t == 6) {
        WAITV(0); BAR();
        if (c < 6) {
#pragma unroll
          for (int j = 0; j < 3; j++) glds16(src + (c + 2) * 24576 + j * 1024, __builtin_amdgcn_readfirstlane(fre) + w * 3072 + j * 1024);
        }
      }
      if (t == 3) {
        if (__builtin_expect(__any(o[12] > 1024.f), 0)) {
          pb[2] = TR2(e0, e1); pb[3] = TR2(e2, e3);
          o = mma32(v1, asbf(pb), o);
          pb = u32x4{0u, 0u, 0u, 0u}; e0 = e1 = e2 = e3 = 0.f;
          float lm = xhalf_max(o[12]);
          int e = __builtin_amdgcn_frexp_expf(lm) + 59;
          float mn = split_ref(mref + (float)(e > 0 ? (e < 100 ? e : 100) : 0), slot);
          float dl = mn - mref, f = EX(-dl);
          mref = mn;
          if (hh) q2[0] = slot;
#pragma unroll
          for (int j = 0; j < 16; j++) { o[j] *= f; s[j] -= dl; }
        }
      }
      u32x4 pa;
#define SLOT(i, P0, P1) { float n0 = EX(s[i]), n1 = EX(s[i + 1]); SB(); P0 = TR2(e0, e1); SB(); float n2 = EX(s[i + 2]); SB(); \
                          P1 = TR2(e2, e3); SB(); e3 = EX(s[i + 3]); e0 = n0; e1 = n1; e2 = n2; SB(); }
      SB();
      f32x16 sn = mma32(k1, q1, zero);
      SB();
      k1 = ldsr128(t < 6 ? ck + (t + 2) * 3072 : nk + (t - 6) * 3072);
      SLOT(0, pb[2], pb[3])
      sn = mma32(asbf(k2), asbf(q2), sn);
      SB();
      { u32x2 t2 = ldsr64(t < 6 ? ck2 + (t + 2) * 3072 : nk2 + (t - 6) * 3072); k2[0] = t2[0]; k2[1] = t2[1]; }
      SLOT(4, pa[0], pa[1])
      o = mma32(v1, asbf(pb), o);
      SB();
      v1 = ldsr128(cv + t * 3072 + 768);
      SLOT(8, pa[2], pa[3])
      o = mma32(v0, asbf(pa), o);
      SB();
      v0 = ldsr128(t < 7 ? cv + (t + 1) * 3072 : nv);
      SLOT(12, pb[0], pb[1])
      s = sn;
    }
    unsigned tmp = cur; cur = nxt; nxt = fre; fre = tmp;
  }
  pb[2] = TR2(e0, e1); pb[3] = TR2(e2, e3);
  o = mma32(v1, asbf(pb), o);
  float chk = o[12];
#pragma unroll
  for (int j = 0; j < 12; j++) chk = fmaxf(chk, fabsf((hh && j >= 8) ? 0.f : o[j]) );
  bool bad = !(chk < 1e37f) || !(o[12] > 0.f);
#pragma unroll
  for (int j = 0; j < 13; j++) bad = bad || ((hh && j >= 8 && j < 12) ? false : (o[j] != o[j]));
  if (__builtin_expect(__any(bad), 0)) {
    if (hh) q2[0] = 0u;
    float mx = -INFINITY;
    o = f32x16{};
    for (int t = 0; t < 64; t++) {
      const char* tb = kvg + t * 3072;
      u32x2 k2 = *(const u32x2*)(tb + kb);
      f32x16 z = {};
      z = mma32(*(const bf16x8*)(tb + ka), q1, z);
      z = mma32(asbf(u32x4{k2[0], k2[1], 0u, 0u}), asbf(q2), z);
      float mn = fmaxf(mx, xhalf_max(max16(z)));
      float f = __builtin_amdgcn_exp2f(mx - mn);
      mx = mn;
#pragma unroll
      for (int j = 0; j < 16; j++) { o[j] *= f; z[j] -= mn; }
      bf16x8 p0, p1;
      packP(z, p0, p1);
      o = mma32(*(const bf16x8*)(tb + va), p0, o);
      o = mma32(*(const bf16x8*)(tb + va + 768), p1, o);
    }
  }
  const float inv = 1.f / o[12];
  char* ab = ws + OFF_A + (size_t)(row >> 4) * (KS * 1024) + (row & 15) * 16;
#pragma unroll
  for (int g = 0; g < 3; g++) {
    if (g < 2 || !hh) {
      const int k = head * 20 + 8 * g + 4 * hh;
      u32x2 ov = {pk2(o[4 * g] * inv, o[4 * g + 1] * inv), pk2(o[4 * g + 2] * inv, o[4 * g + 3] * inv)};
      *(u32x2*)(ab + (k >> 3) * 256 + ((k >> 2) & 1) * 8) = ov;
    }
  }
}

__device__ __forceinline__ void gu_body(int id, const u16* __restrict__ wg, const u16* __restrict__ wu, char* __restrict__ ws, unsigned sb) {
  const int tid = threadIdx.x, l = tid & 63, w = __builtin_amdgcn_readfirstlane(tid >> 6);
  const int hh = l >> 5, m = l & 31;
  const int T = (id & 7) * 27 + (id >> 3), nt = T >> 3, mt = T & 7;
  const char* xs = ws + OFF_XN + (size_t)(mt * 8 + w) * 40960 + l * 16;
  const u16* wsrc = ((w & 1) ? wu : wg) + (size_t)((w >> 1) * 8 + ((l & 31) >> 2)) * F_ + nt * 64 + (l >> 5) * 32 + (l & 3) * 8;
  auto issue = [&](int st, unsigned buf) {
    glds16(xs + st * 2048, buf + w * 2048);
    glds16(xs + st * 2048 + 1024, buf + w * 2048 + 1024);
    glds16(wsrc + (size_t)st * 32 * F_, buf + 16384 + w * 1024);
  };
  issue(0, sb); issue(1, sb + 24576);
  const int wm = w & 3, wn = w >> 2;
  const unsigned xa = wm * 4096 + l * 16;
  const unsigned wa = 16384 + hh * 2048 + wn * 512 + ((l & 15) >> 2) * 64 + ((l >> 4) & 1) * 32 + (l & 3) * 8;
  f32x16 acc[2][2] = {};
  unsigned cur = sb, nxt = sb + 24576, fre = sb + 49152;
  for (int st = 0; st < 20; st++) {
    if (st < 19) WAITV(3); else WAITV(0);
    BAR();
    if (st < 18) issue(st + 2, __builtin_amdgcn_readfirstlane(fre));
#pragma unroll
    for (int s = 0; s < 2; s++) {
      bf16x8 x0 = ldsr128(cur + xa + s * 1024), x1 = ldsr128(cur + xa + 2048 + s * 1024);
      bf16x8 g = ldstr(cur + wa + s * 4096, cur + wa + s * 4096 + 256);
      bf16x8 u = ldstr(cur + wa + s * 4096 + 1024, cur + wa + s * 4096 + 1024 + 256);
      acc[0][0] = mma32(g, x0, acc[0][0]); acc[0][1] = mma32(g, x1, acc[0][1]);
      acc[1][0] = mma32(u, x0, acc[1][0]); acc[1][1] = mma32(u, x1, acc[1][1]);
    }
    unsigned tmp = cur; cur = nxt; nxt = fre; fre = tmp;
  }
#pragma unroll
  for (int mb = 0; mb < 2; mb++) {
    const int row = mt * 256 + wm * 64 + mb * 32 + m;
    char* ab = ws + OFF_A + (size_t)(row >> 4) * (KS * 1024) + (20 + nt * 2 + wn) * 1024 + (row & 15) * 16 + hh * 8;
#pragma unroll
    for (int g = 0; g < 4; g++) {
      float h[4];
#pragma unroll
      for (int i = 0; i < 4; i++) {
        float gv = acc[0][mb][4 * g + i], uv = acc[1][mb][4 * g + i];
        h[i] = gv * __builtin_amdgcn_rcpf(1.f + __builtin_amdgcn_exp2f(gv * -1.4426950408889634f)) * uv;
      }
      *(u32x2*)(ab + g * 256) = u32x2{pk2(h[0], h[1]), pk2(h[2], h[3])};
    }
  }
}

__device__ __forceinline__ void pack_body(int id, const u16* __restrict__ wo, const u16* __restrict__ wd, char* __restrict__ ws) {
  for (int u = id * 512 + threadIdx.x; u < 296 * 80; u += N_PACK * 512) {
    const int ko = u / 80, no = u - ko * 80;
    const u16* src = (ko < 80 ? wo + (size_t)ko * 8 * D_ : wd + (size_t)(ko - 80) * 8 * D_) + no * 8;
    u32x4 r[8];
#pragma unroll
    for (int i = 0; i < 8; i++) r[i] = *(const u32x4*)(src + i * D_);
    char* dst = ws + OFF_W + (size_t)(no >> 1) * (KS * 1024) + ko * 256 + (no & 1) * 128;
#pragma unroll
    for (int j = 0; j < 4; j++) {
      u32x4 e, o;
#pragma unroll
      for (int i = 0; i < 4; i++) {
        e[i] = __builtin_amdgcn_perm(r[2 * i + 1][j], r[2 * i][j], 0x05040100u);
        o[i] = __builtin_amdgcn_perm(r[2 * i + 1][j], r[2 * i][j], 0x07060302u);
      }
      *(u32x4*)(dst + (2 * j) * 16) = e;
      *(u32x4*)(dst + (2 * j + 1) * 16) = o;
    }
  }
}

__global__ void __launch_bounds__(512, 4) k2_mix(const u16* __restrict__ wg, const u16* __restrict__ wu, const u16* __restrict__ wo,
                                                 const u16* __restrict__ wd, char* __restrict__ ws) {
  __shared__ __attribute__((aligned(16))) char smem[K2_LDS];
  const unsigned sb = (unsigned)(uintptr_t)(LAS char*)smem;
  const int b = blockIdx.x;
  if (b < N_ATT) attn_body(b, ws, sb);
  else if (b < N_ATT + N_GU) gu_body(b - N_ATT, wg, wu, ws, sb);
  else pack_body(b - N_ATT - N_GU, wo, wd, ws);
}

constexpr int K3_DEPTH = 5;
__global__ void __launch_bounds__(256, 1) k3_out(const u16* __restrict__ x, const char* __restrict__ ws, u16* __restrict__ out) {
  __shared__ __attribute__((aligned(16))) float red[4 * 64 * 84];
  const int tid = threadIdx.x, l = tid & 63, w = __builtin_amdgcn_readfirstlane(tid >> 6);
  const int b = blockIdx.x, xcd = b & 7, j = b >> 3;
  const int mt = (xcd >> 1) * 8 + (j & 7), nt = (xcd & 1) * 4 + (j >> 3);
  const char* ap = ws + OFF_A + (size_t)mt * 4 * (KS * 1024) + l * 16;
  const char* bp = ws + OFF_W + (size_t)nt * 5 * (KS * 1024) + l * 16;
  bf16x8 fa[K3_DEPTH][4], fb[K3_DEPTH][5];
  f32x4 acc[4][5] = {};
  auto load = [&](int i, int slot) {
    int ks = w + 4 * i; ks = ks < KS ? ks : KS - 1;
#pragma unroll
    for (int t = 0; t < 5; t++) fb[slot][t] = *(const bf16x8*)(bp + (size_t)(t * KS + ks) * 1024);
#pragma unroll
    for (int t = 0; t < 4; t++) fa[slot][t] = *(const bf16x8*)(ap + (size_t)(t * KS + ks) * 1024);
  };
#pragma unroll
  for (int i = 0; i < K3_DEPTH; i++) { load(i, i); SB(); }
  const int er = tid >> 2, ec = (tid & 3) * 20;
  const size_t eo = (size_t)(mt * 64 + er) * D_ + nt * 80 + ec;
  u32x2 xr[5];
#pragma unroll
  for (int i = 0; i < 5; i++) xr[i] = *(const u32x2*)(x + eo + 4 * i);
  SB();
#pragma unroll
  for (int i = 0; i < 19; i++) {
    const int slot = i % K3_DEPTH;
    if (i < 18 || w < 2) {
#pragma unroll
      for (int a = 0; a < 4; a++)
#pragma unroll
        for (int t = 0; t < 5; t++) acc[a][t] = mma16(fb[slot][t], fa[slot][a], acc[a][t]);
    }
    SB();
    if (i + K3_DEPTH < 19) load(i + K3_DEPTH, slot);
    SB();
  }
  float* rw = red + w * (64 * 84);
#pragma unroll
  for (int a = 0; a < 4; a++)
#pragma unroll
    for (int t = 0; t < 5; t++) *(f32x4*)(rw + (a * 16 + (l & 15)) * 84 + t * 16 + (l >> 4) * 4) = acc[a][t];
  __syncthreads();
#pragma unroll
  for (int i = 0; i < 5; i++) {
    const float* rp = red + er * 84 + ec + 4 * i;
    f32x4 v = *(const f32x4*)rp + *(const f32x4*)(rp + 64 * 84) + *(const f32x4*)(rp + 2 * 64 * 84) + *(const f32x4*)(rp + 3 * 64 * 84);
    u32x2 o = {pk2(v[0] + lo16(xr[i][0]), v[1] + hi16(xr[i][0])), pk2(v[2] + lo16(xr[i][1]), v[3] + hi16(xr[i][1]))};
    *(u32x2*)(out + eo + 4 * i) = o;
  }
}

extern "C" void kernel_launch(void* const* d_in, const int* in_sizes, int n_in, void* d_out, int out_size, void* d_ws, size_t ws_size,
                              hipStream_t stream) {
  const u16* x = (const u16*)d_in[0];
  char* ws = (char*)d_ws;
  k1_ln_qkv<<<256, 512, 0, stream>>>(x, (const u16*)d_in[1], (const u16*)d_in[2], (const u16*)d_in[3], ws);
  k2_mix<<<N_ATT + N_GU + N_PACK, 512, 0, stream>>>((const u16*)d_in[6], (const u16*)d_in[7], (const u16*)d_in[4], (const u16*)d_in[8], ws);
  k3_out<<<256, 256, 0, stream>>>(x, ws, (u16*)d_out);
}
```

```cpp
#include <hip/hip_runtime.h>
#include <hip/hip_bf16.h>
#include <stdint.h>

typedef __attribute__((ext_vector_type(8))) __bf16 bf16x8;
typedef __attribute__((ext_vector_type(2))) __bf16 bf16x2;
typedef __attribute__((ext_vector_type(4))) short s16x4;
typedef __attribute__((ext_vector_type(16))) float f32x16;
typedef __attribute__((ext_vector_type(4))) float f32x4;
typedef __attribute__((ext_vector_type(2))) float f32x2;
typedef __attribute__((ext_vector_type(4))) unsigned u32x4;
typedef __attribute__((ext_vector_type(2))) unsigned u32x2;
#define LAS __attribute__((address_space(3)))
typedef unsigned short u16;

constexpr int S_ = 2048, D_ = 640, F_ = 1728;
constexpr int KS = 74;
constexpr size_t OFF_XN = 0;
constexpr size_t OFF_Q = OFF_XN + 2621440;
constexpr size_t OFF_KV = OFF_Q + 2621440;
constexpr size_t OFF_A = OFF_KV + 196608;
constexpr size_t OFF_W = OFF_A + 9699328;

__device__ __forceinline__ void glds16(const void* g, unsigned lds) {
  unsigned keep;
  asm volatile("s_mov_b32 %0, m0\n\ts_mov_b32 m0, %2\n\ts_nop 0\n\tglobal_load_lds_dwordx4 %1, off\n\ts_mov_b32 m0, %0"
               : "=&s"(keep) : "v"(g), "s"(lds) : "memory");
}
__device__ __forceinline__ unsigned pk2(float a, float b) {
  bf16x2 v = {(__bf16)a, (__bf16)b};
  return __builtin_bit_cast(unsigned, v);
}
__device__ __forceinline__ float bfr(float a) { return (float)(__bf16)a; }
__device__ __forceinline__ float lo16(unsigned u) { return __uint_as_float(u << 16); }
__device__ __forceinline__ float hi16(unsigned u) { return __uint_as_float(u & 0xffff0000u); }
__device__ __forceinline__ bf16x8 ldsr128(unsigned a) { return *(const LAS bf16x8*)(uintptr_t)a; }
__device__ __forceinline__ u32x2 ldsr64(unsigned a) { return *(const LAS u32x2*)(uintptr_t)a; }
__device__ __forceinline__ bf16x8 ldstr(unsigned a0, unsigned a1) {
  s16x4 x = __builtin_amdgcn_ds_read_tr16_b64_v4i16((LAS s16x4*)(uintptr_t)a0);
  s16x4 y = __builtin_amdgcn_ds_read_tr16_b64_v4i16((LAS s16x4*)(uintptr_t)a1);
  u32x2 xa = __builtin_bit_cast(u32x2, x), ya = __builtin_bit_cast(u32x2, y);
  u32x4 r = {xa[0], xa[1], ya[0], ya[1]};
  return __builtin_bit_cast(bf16x8, r);
}
__device__ __forceinline__ f32x16 mma32(bf16x8 a, bf16x8 b, f32x16 c) { return __builtin_amdgcn_mfma_f32_32x32x16_bf16(a, b, c, 0, 0, 0); }
__device__ __forceinline__ f32x4 mma16(bf16x8 a, bf16x8 b, f32x4 c) { return __builtin_amdgcn_mfma_f32_16x16x32_bf16(a, b, c, 0, 0, 0); }
__device__ __forceinline__ bf16x8 asbf(u32x4 v) { return __builtin_bit_cast(bf16x8, v); }
__device__ __forceinline__ float xhalf_max(float v) {
  auto r = __builtin_amdgcn_permlane32_swap(__float_as_uint(v), __float_as_uint(v), false, false);
  return fmaxf(__uint_as_float(r[0]), __uint_as_float(r[1]));
}
#define WAITV(n) asm volatile("s_waitcnt vmcnt(" #n ")" ::: "memory")
#define BAR() asm volatile("s_waitcnt lgkmcnt(0)\n\ts_barrier" ::: "memory")

#define SB() __builtin_amdgcn_sched_barrier(0)
__device__ __forceinline__ float row16_sum(float v) {
  v += __builtin_bit_cast(float, __builtin_amdgcn_update_dpp(0, __builtin_bit_cast(int, v), 0x128, 0xf, 0xf, false));
  v += __builtin_bit_cast(float, __builtin_amdgcn_update_dpp(0, __builtin_bit_cast(int, v), 0x124, 0xf, 0xf, false));
  v += __builtin_bit_cast(float, __builtin_amdgcn_update_dpp(0, __builtin_bit_cast(int, v), 0x122, 0xf, 0xf, false));
  v += __builtin_bit_cast(float, __builtin_amdgcn_update_dpp(0, __builtin_bit_cast(int, v), 0x121, 0xf, 0xf, false));
  return v;
}
__device__ __forceinline__ void pack_unit(int u, const u16* __restrict__ wo, const u16* __restrict__ wd, const u16* __restrict__ wq,
                                          char* __restrict__ ws) {
  const int ko = u / 80, no = u - ko * 80;
  const u16* src = (ko < 80 ? wo + (size_t)ko * 8 * D_ : (ko < 296 ? wd + (size_t)(ko - 80) * 8 * D_ : wq + (size_t)(ko - 296) * 8 * D_)) + no * 8;
  u32x4 r[8];
#pragma unroll
  for (int i = 0; i < 8; i++) r[i] = *(const u32x4*)(src + i * D_);
  char* dst = ws + OFF_W + (size_t)(no >> 1) * (KS * 1024) + ko * 256 + (no & 1) * 128;
#pragma unroll
  for (int j = 0; j < 4; j++) {
    u32x4 e, o;
#pragma unroll
    for (int i = 0; i < 4; i++) {
      e[i] = __builtin_amdgcn_perm(r[2 * i + 1][j], r[2 * i][j], 0x05040100u);
      o[i] = __builtin_amdgcn_perm(r[2 * i + 1][j], r[2 * i][j], 0x07060302u);
    }
    const int n = no * 8 + 2 * j, hd = n / 20;
    char* d2 = ko < 296 ? dst + j * 32 : ws + OFF_Q + hd * 25600 + (ko - 296) * 320 + (n - hd * 20) * 16;
    *(u32x4*)d2 = e;
    *(u32x4*)(d2 + 16) = o;
  }
}
constexpr int K1_KV = 40960, K1_RED = K1_KV + 25600 + 128, K1_LDS = K1_RED + 3 * 4096;
__global__ void __launch_bounds__(512, 2) k1_ln_kv(const u16* __restrict__ x, const u16* __restrict__ wk, const u16* __restrict__ wv,
                                                   const u16* __restrict__ wq, char* __restrict__ ws) {
  __shared__ __attribute__((aligned(16))) char smem[K1_LDS];
  const unsigned sb = (unsigned)(uintptr_t)(LAS char*)smem;
  const int tid = threadIdx.x, l = tid & 63, w = __builtin_amdgcn_readfirstlane(tid >> 6);
  const int b = blockIdx.x;
  if (b >= 128) {
    if (tid < 50) pack_unit(296 * 80 + (b - 128) * 50 + tid, wq, wq, wq, ws);
    return;
  }
  const int cg = b & 1, rb = b >> 1, r = tid >> 4, p = tid & 15;
  u32x4 xv[5];
  const u16* xp = x + (size_t)(rb * 32 + r) * D_ + p * 8;
#pragma unroll
  for (int i = 0; i < 5; i++) xv[i] = *(const u32x4*)(xp + i * 128);
  SB();
  {
    const u16* src = (cg == 0 ? wk : wv) + l * 8;
#pragma unroll
    for (int j = 0; j < 3; j++) glds16(src + (w * 3 + j) * 512, sb + K1_KV + (w * 3 + j) * 1024);
    if (w == 7) glds16(src + 24 * 512, sb + K1_KV + 24 * 1024);
  }
  float s = 0.f, s1 = 0.f;
  const bf16x2 one2 = {(__bf16)1.f, (__bf16)1.f};
#pragma unroll
  for (int i = 0; i < 5; i++)
#pragma unroll
    for (int j = 0; j < 4; j += 2) {
      const unsigned u0 = xv[i][j], u1 = xv[i][j + 1];
      s = __builtin_amdgcn_fdot2_f32_bf16(__builtin_bit_cast(bf16x2, u0), one2, s, false);
      s1 = __builtin_amdgcn_fdot2_f32_bf16(__builtin_bit_cast(bf16x2, u1), one2, s1, false);
    }
  const float mean = row16_sum(s + s1) * (1.f / D_), mb = bfr(mean);
  f32x2 v[20], qa = {0.f, 0.f}, qb = {0.f, 0.f};
#pragma unroll
  for (int i = 0; i < 20; i++) {
    v[i] = f32x2{lo16(xv[i >> 2][i & 3]), hi16(xv[i >> 2][i & 3])} - f32x2{mb, mb};
    if (i & 1) qb += v[i] * v[i]; else qa += v[i] * v[i];
  }
  qa += qb;
  const float dm = mean - mb, vb = bfr(fmaxf(row16_sum(qa[0] + qa[1]) * (1.f / D_) - dm * dm, 0.f));
  const float rs = __builtin_amdgcn_rcpf(bfr(__builtin_amdgcn_sqrtf(bfr(vb + 1e-5f))));
  char* xnf = ws + OFF_XN + (size_t)rb * 40960;
  WAITV(0);
#pragma unroll
  for (int i = 0; i < 5; i++) {
    u32x4 o;
#pragma unroll
    for (int j = 0; j < 4; j++) { f32x2 t = v[i * 4 + j] * f32x2{rs, rs}; o[j] = pk2(t[0], t[1]); }
    const int c = p + 16 * i;
    *(LAS u32x4*)(uintptr_t)(sb + c * 512 + ((r ^ (c & 7)) << 4)) = o;
    if (i == 4 ? ((p >> 3) == cg) : ((i >> 1) == cg)) *(u32x4*)(xnf + c * 512 + r * 16) = o;
  }
  BAR();
  const int hh = l >> 5, m = l & 31;
  f32x16 acc = {};
  if (w < 4) {
    const unsigned ta = sb + K1_KV + (hh * 8 + ((l & 15) >> 2)) * 40 + ((l >> 4) & 1) * 32 + (l & 3) * 8 + w * 6400;
    bf16x8 xf[10], wf[10];
#pragma unroll
    for (int k = 0; k < 10; k++) {
      const int c = (w * 10 + k) * 2 + hh;
      xf[k] = ldsr128(sb + c * 512 + ((m ^ (c & 7)) << 4));
      wf[k] = ldstr(ta + k * 640, ta + k * 640 + 160);
    }
    if (cg == 0) {
#pragma unroll
      for (int k = 0; k < 10; k++) acc = mma32(wf[k], xf[k], acc);
    } else {
#pragma unroll
      for (int k = 0; k < 10; k++) acc = mma32(xf[k], wf[k], acc);
    }
    if (w > 0) {
#pragma unroll
      for (int g = 0; g < 4; g++)
        *(LAS f32x4*)(uintptr_t)(sb + K1_RED + (w - 1) * 4096 + g * 1024 + l * 16) = f32x4{acc[4 * g], acc[4 * g + 1], acc[4 * g + 2], acc[4 * g + 3]};
    }
  }
  BAR();
  if (w == 0) {
#pragma unroll
    for (int q = 0; q < 3; q++)
#pragma unroll
      for (int g = 0; g < 4; g++) {
        f32x4 t = *(const LAS f32x4*)(uintptr_t)(sb + K1_RED + q * 4096 + g * 1024 + l * 16);
#pragma unroll
        for (int i = 0; i < 4; i++) acc[4 * g + i] += t[i];
      }
    char* tile = ws + OFF_KV + (size_t)rb * 3072;
    if (cg == 0) {
      u32x4 o = {pk2(acc[0], acc[1]), pk2(acc[2], acc[3]), pk2(acc[4], acc[5]), pk2(acc[6], acc[7])};
      *(u32x4*)(tile + l * 16) = o;
      u32x2 o2 = {hh ? 0x3f803f80u : pk2(acc[8], acc[9]), hh ? 0u : pk2(acc[10], acc[11])};
      *(u32x2*)(tile + 1024 + l * 8) = o2;
    } else if (m < 24) {
#pragma unroll
      for (int s2 = 0; s2 < 2; s2++) {
        u32x4 o = {pk2(acc[8 * s2], acc[8 * s2 + 1]), pk2(acc[8 * s2 + 2], acc[8 * s2 + 3]), pk2(acc[8 * s2 + 4], acc[8 * s2 + 5]), pk2(acc[8 * s2 + 6], acc[8 * s2 + 7])};
        if (m >= 20) o = u32x4{0x3f803f80u, 0x3f803f80u, 0x3f803f80u, 0x3f803f80u};
        *(u32x4*)(tile + 1536 + (s2 * 2 + hh) * 384 + m * 16) = o;
      }
    }
  }
}

constexpr int K2_LDS = 3 * 24576;
constexpr int N_ATT = 256, N_GU = 216, N_PACK = 40;

__device__ __forceinline__ void packP(const f32x16& s, bf16x8& p0, bf16x8& p1) {
  u32x4 a, b;
#pragma unroll
  for (int j = 0; j < 4; j++) {
    a[j] = pk2(__builtin_amdgcn_exp2f(s[2 * j]), __builtin_amdgcn_exp2f(s[2 * j + 1]));
    b[j] = pk2(__builtin_amdgcn_exp2f(s[8 + 2 * j]), __builtin_amdgcn_exp2f(s[8 + 2 * j + 1]));
  }
  p0 = asbf(a); p1 = asbf(b);
}
__device__ __forceinline__ float max16(const f32x16& s) {
  float a = fmaxf(fmaxf(s[0], s[1]), fmaxf(s[2], s[3]));
#pragma unroll
  for (int j = 4; j < 16; j += 2) a = fmaxf(a, fmaxf(s[j], s[j + 1]));
  return a;
}
__device__ __forceinline__ float split_ref(float mv, unsigned& slot) {
  float hi = bfr(mv), lo = bfr(mv - hi);
  slot = pk2(-hi, -lo);
  return hi + lo;
}

__device__ __forceinline__ void attn_body(int a, char* __restrict__ ws, unsigned sb) {
  const int tid = threadIdx.x, l = tid & 63, w = __builtin_amdgcn_readfirstlane(tid >> 6);
  const int hh = l >> 5, m = l & 31;
  const int qb = (a & 1) * 32 + (a >> 3), hg = (a >> 1) & 3, head = hg * 8 + w;
  const char* kvg = ws + OFF_KV;
  const char* src = kvg + w * 3072 + l * 16;
  const int row = qb * 32 + m;
  bf16x8 q1;
  u32x4 q2;
  {
    const char* wp = ws + OFF_Q + head * 25600 + hh * 320 + (m < 20 ? m : 19) * 16;
    bf16x8 wf[12], xf[2][4];
#pragma unroll
    for (int k = 0; k < 12; k++) wf[k] = *(const bf16x8*)(wp + k * 640);
    SB();
#pragma unroll
    for (int j = 0; j < 5; j++) glds16(ws + OFF_XN + (size_t)qb * 40960 + (w * 5 + j) * 1024 + l * 16, sb + (w * 5 + j) * 1024);
#pragma unroll
    for (int j = 0; j < 3; j++) glds16(src + j * 1024, sb + 49152 + w * 3072 + j * 1024);
    WAITV(3); BAR();
    f32x16 acc = {};
#pragma unroll
    for (int i = 0; i < 4; i++) xf[0][i] = ldsr128(sb + i * 1024 + l * 16);
#pragma unroll
    for (int g = 0; g < 10; g++) {
      SB();
      if (g < 9) {
#pragma unroll
        for (int i = 0; i < 4; i++) xf[(g + 1) & 1][i] = ldsr128(sb + (g * 4 + 4 + i) * 1024 + l * 16);
      }
      SB();
#pragma unroll
      for (int i = 0; i < 4; i++) {
        const int k = g * 4 + i;
        acc = mma32(wf[k % 12], xf[g & 1][i], acc);
        SB();
        if (k + 12 < 40) wf[k % 12] = *(const bf16x8*)(wp + (k + 12) * 640);
      }
    }
    const float sc = 0.22360679774997896f * 1.4426950408889634f;
    unsigned qq[6];
#pragma unroll
    for (int j = 0; j < 6; j++) qq[j] = pk2(bfr(acc[2 * j]) * sc, bfr(acc[2 * j + 1]) * sc);
    q1 = asbf(u32x4{qq[0], qq[1], qq[2], qq[3]});
    q2 = u32x4{hh ? 0u : qq[4], hh ? 0u : qq[5], 0u, 0u};
  }
  unsigned cur = sb + 49152, nxt = sb, fre = sb + 24576;
  const unsigned ka = l * 16, kb = 1024 + l * 8, va = 1536 + hh * 384 + (m < 20 ? m : 20) * 16;
  WAITV(0); BAR();
#pragma unroll
  for (int j = 0; j < 3; j++) glds16(src + 24576 + j * 1024, sb + w * 3072 + j * 1024);
#define EX(v) __builtin_amdgcn_exp2f(v)
#define TR2(a, b) __builtin_amdgcn_perm(__float_as_uint(b), __float_as_uint(a), 0x07060302u)
  const f32x16 zero = {};
  u32x4 pb = {0u, 0u, 0u, 0u};
  float e0 = 0.f, e1 = 0.f, e2 = 0.f, e3 = 0.f;
  __builtin_amdgcn_s_setprio(2);
  bf16x8 k1 = ldsr128(cur + ka), v0 = ldsr128(cur + va), v1 = ldsr128(cur + va + 768);
  u32x4 k2 = {0u, 0u, 0u, 0u};
  { u32x2 t2 = ldsr64(cur + kb); k2[0] = t2[0]; k2[1] = t2[1]; }
  f32x16 s = mma32(asbf(k2), asbf(q2), mma32(k1, q1, zero)), o = {};
  k1 = ldsr128(cur + 3072 + ka);
  { u32x2 t2 = ldsr64(cur + 3072 + kb); k2[0] = t2[0]; k2[1] = t2[1]; }
  unsigned slot;
  float mref = split_ref(xhalf_max(max16(s)) + 60.f, slot);
  if (hh) q2[0] = slot;
#pragma unroll
  for (int j = 0; j < 16; j++) s[j] -= mref;
  for (int c = 0; c < 8; c++) {
    const unsigned ck = cur + ka, ck2 = cur + kb, cv = cur + va, nk = nxt + ka, nk2 = nxt + kb, nv = nxt + va;
#pragma unroll
    for (int t = 0; t < 8; t++) {
      if (t == 6) {
        WAITV(0); BAR();
        if (c < 6) {
#pragma unroll
          for (int j = 0; j < 3; j++) glds16(src + (c + 2) * 24576 + j * 1024, __builtin_amdgcn_readfirstlane(fre) + w * 3072 + j * 1024);
        }
      }
      if (t == 3) {
        if (__builtin_expect(__any(o[12] > 1024.f), 0)) {
          pb[2] = TR2(e0, e1); pb[3] = TR2(e2, e3);
          o = mma32(v1, asbf(pb), o);
          pb = u32x4{0u, 0u, 0u, 0u}; e0 = e1 = e2 = e3 = 0.f;
          float lm = xhalf_max(o[12]);
          int e = __builtin_amdgcn_frexp_expf(lm) + 59;
          float mn = split_ref(mref + (float)(e > 0 ? (e < 100 ? e : 100) : 0), slot);
          float dl = mn - mref, f = EX(-dl);
          mref = mn;
          if (hh) q2[0] = slot;
#pragma unroll
          for (int j = 0; j < 16; j++) { o[j] *= f; s[j] -= dl; }
        }
      }
      u32x4 pa;
#define SLOT(i, P0, P1) { float n0 = EX(s[i]), n1 = EX(s[i + 1]); SB(); P0 = TR2(e0, e1); SB(); float n2 = EX(s[i + 2]); SB(); \
                          P1 = TR2(e2, e3); SB(); e3 = EX(s[i + 3]); e0 = n0; e1 = n1; e2 = n2; SB(); }
      SB();
      f32x16 sn = mma32(k1, q1, zero);
      SB();
      k1 = ldsr128(t < 6 ? ck + (t + 2) * 3072 : nk + (t - 6) * 3072);
      SLOT(0, pb[2], pb[3])
      sn = mma32(asbf(k2), asbf(q2), sn);
      SB();
      { u32x2 t2 = ldsr64(t < 6 ? ck2 + (t + 2) * 3072 : nk2 + (t - 6) * 3072); k2[0] = t2[0]; k2[1] = t2[1]; }
      SLOT(4, pa[0], pa[1])
      o = mma32(v1, asbf(pb), o);
      SB();
      v1 = ldsr128(cv + t * 3072 + 768);
      SLOT(8, pa[2], pa[3])
      o = mma32(v0, asbf(pa), o);
      SB();
      v0 = ldsr128(t < 7 ? cv + (t + 1) * 3072 : nv);
      SLOT(12, pb[0], pb[1])
      s = sn;
    }
    unsigned tmp = cur; cur = nxt; nxt = fre; fre = tmp;
  }
  pb[2] = TR2(e0, e1); pb[3] = TR2(e2, e3);
  o = mma32(v1, asbf(pb), o);
  float chk = o[12];
#pragma unroll
  for (int j = 0; j < 12; j++) chk = fmaxf(chk, fabsf((hh && j >= 8) ? 0.f : o[j]) );
  bool bad = !(chk < 1e37f) || !(o[12] > 0.f);
#pragma unroll
  for (int j = 0; j < 13; j++) bad = bad || ((hh && j >= 8 && j < 12) ? false : (o[j] != o[j]));
  if (__builtin_expect(__any(bad), 0)) {
    if (hh) q2[0] = 0u;
    float mx = -INFINITY;
    o = f32x16{};
    for (int t = 0; t < 64; t++) {
      const char* tb = kvg + t * 3072;
      u32x2 k2 = *(const u32x2*)(tb + kb);
      f32x16 z = {};
      z = mma32(*(const bf16x8*)(tb + ka), q1, z);
      z = mma32(asbf(u32x4{k2[0], k2[1], 0u, 0u}), asbf(q2), z);
      float mn = fmaxf(mx, xhalf_max(max16(z)));
      float f = __builtin_amdgcn_exp2f(mx - mn);
      mx = mn;
#pragma unroll
      for (int j = 0; j < 16; j++) { o[j] *= f; z[j] -= mn; }
      bf16x8 p0, p1;
      packP(z, p0, p1);
      o = mma32(*(const bf16x8*)(tb + va), p0, o);
      o = mma32(*(const bf16x8*)(tb + va + 768), p1, o);
    }
  }
  const float inv = 1.f / o[12];
  char* ab = ws + OFF_A + (size_t)(row >> 4) * (KS * 1024) + (row & 15) * 16;
#pragma unroll
  for (int g = 0; g < 3; g++) {
    if (g < 2 || !hh) {
      const int k = head * 20 + 8 * g + 4 * hh;
      u32x2 ov = {pk2(o[4 * g] * inv, o[4 * g + 1] * inv), pk2(o[4 * g + 2] * inv, o[4 * g + 3] * inv)};
      *(u32x2*)(ab + (k >> 3) * 256 + ((k >> 2) & 1) * 8) = ov;
    }
  }
}

__device__ __forceinline__ void gu_body(int id, const u16* __restrict__ wg, const u16* __restrict__ wu, char* __restrict__ ws, unsigned sb) {
  const int tid = threadIdx.x, l = tid & 63, w = __builtin_amdgcn_readfirstlane(tid >> 6);
  const int hh = l >> 5, m = l & 31;
  const int T = ((id >> 1) & 3) * 27 + (id >> 3), nt = T >> 2, mt = (id & 1) * 4 + (T & 3);
  const char* xs = ws + OFF_XN + (size_t)(mt * 8 + w) * 40960 + l * 16;
  const u16* wsrc = ((w & 1) ? wu : wg) + (size_t)((w >> 1) * 8 + ((l & 31) >> 2)) * F_ + nt * 64 + (l >> 5) * 32 + (l & 3) * 8;
  auto issue = [&](int st, unsigned buf) {
    glds16(xs + st * 2048, buf + w * 2048);
    glds16(xs + st * 2048 + 1024, buf + w * 2048 + 1024);
    glds16(wsrc + (size_t)st * 32 * F_, buf + 16384 + w * 1024);
  };
  issue(0, sb); issue(1, sb + 24576);
  const int wm = w & 3, wn = w >> 2;
  const unsigned xa = wm * 4096 + l * 16;
  const unsigned wa = 16384 + hh * 2048 + wn * 512 + ((l & 15) >> 2) * 64 + ((l >> 4) & 1) * 32 + (l & 3) * 8;
  f32x16 acc[2][2] = {};
  unsigned cur = sb, nxt = sb + 24576, fre = sb + 49152;
  for (int st = 0; st < 20; st++) {
    if (st < 19) WAITV(3); else WAITV(0);
    BAR();
    if (st < 18) issue(st + 2, __builtin_amdgcn_readfirstlane(fre));
#pragma unroll
    for (int s = 0; s < 2; s++) {
      bf16x8 x0 = ldsr128(cur + xa + s * 1024), x1 = ldsr128(cur + xa + 2048 + s * 1024);
      bf16x8 g = ldstr(cur + wa + s * 4096, cur + wa + s * 4096 + 256);
      bf16x8 u = ldstr(cur + wa + s * 4096 + 1024, cur + wa + s * 4096 + 1024 + 256);
      acc[0][0] = mma32(g, x0, acc[0][0]); acc[0][1] = mma32(g, x1, acc[0][1]);
      acc[1][0] = mma32(u, x0, acc[1][0]); acc[1][1] = mma32(u, x1, acc[1][1]);
    }
    unsigned tmp = cur; cur = nxt; nxt = fre; fre = tmp;
  }
#pragma unroll
  for (int mb = 0; mb < 2; mb++) {
    const int row = mt * 256 + wm * 64 + mb * 32 + m;
    char* ab = ws + OFF_A + (size_t)(row >> 4) * (KS * 1024) + (20 + nt * 2 + wn) * 1024 + (row & 15) * 16 + hh * 8;
#pragma unroll
    for (int g = 0; g < 4; g++) {
      float h[4];
#pragma unroll
      for (int i = 0; i < 4; i++) {
        float gv = acc[0][mb][4 * g + i], uv = acc[1][mb][4 * g + i];
        h[i] = gv * __builtin_amdgcn_rcpf(1.f + __builtin_amdgcn_exp2f(gv * -1.4426950408889634f)) * uv;
      }
      *(u32x2*)(ab + g * 256) = u32x2{pk2(h[0], h[1]), pk2(h[2], h[3])};
    }
  }
}

__global__ void __launch_bounds__(512, 4) k2_mix(const u16* __restrict__ wg, const u16* __restrict__ wu, const u16* __restrict__ wo,
                                                 const u16* __restrict__ wd, char* __restrict__ ws) {
  __shared__ __attribute__((aligned(16))) char smem[K2_LDS];
  const unsigned sb = (unsigned)(uintptr_t)(LAS char*)smem;
  const int b = blockIdx.x;
  if (b < N_ATT) attn_body(b, ws, sb);
  else if (b < N_ATT + N_GU) gu_body(b - N_ATT, wg, wu, ws, sb);
  else
    for (int u = (b - N_ATT - N_GU) * 512 + threadIdx.x; u < 296 * 80; u += N_PACK * 512) pack_unit(u, wo, wd, wd, ws);
}

constexpr int K3_DEPTH = 5;
__global__ void __launch_bounds__(256, 1) k3_out(const u16* __restrict__ x, const char* __restrict__ ws, u16* __restrict__ out) {
  __shared__ __attribute__((aligned(16))) float red[4 * 64 * 84];
  const int tid = threadIdx.x, l = tid & 63, w = __builtin_amdgcn_readfirstlane(tid >> 6);
  const int b = blockIdx.x, xcd = b & 7, j = b >> 3;
  const int mt = (xcd >> 1) * 8 + (j & 7), nt = (xcd & 1) * 4 + (j >> 3);
  const char* ap = ws + OFF_A + (size_t)mt * 4 * (KS * 1024) + l * 16;
  const char* bp = ws + OFF_W + (size_t)nt * 5 * (KS * 1024) + l * 16;
  bf16x8 fa[K3_DEPTH][4], fb[K3_DEPTH][5];
  f32x4 acc[4][5] = {};
  auto load = [&](int i, int slot) {
    int ks = w + 4 * i; ks = ks < KS ? ks : KS - 1;
#pragma unroll
    for (int t = 0; t < 5; t++) fb[slot][t] = *(const bf16x8*)(bp + (size_t)(t * KS + ks) * 1024);
#pragma unroll
    for (int t = 0; t < 4; t++) fa[slot][t] = *(const bf16x8*)(ap + (size_t)(t * KS + ks) * 1024);
  };
#pragma unroll
  for (int i = 0; i < K3_DEPTH; i++) { load(i, i); SB(); }
  const int er = tid >> 2, ec = (tid & 3) * 20;
  const size_t eo = (size_t)(mt * 64 + er) * D_ + nt * 80 + ec;
  u32x2 xr[5];
#pragma unroll
  for (int i = 0; i < 5; i++) xr[i] = *(const u32x2*)(x + eo + 4 * i);
  SB();
#pragma unroll
  for (int i = 0; i < 19; i++) {
    const int slot = i % K3_DEPTH;
    if (i < 18 || w < 2) {
#pragma unroll
      for (int a = 0; a < 4; a++)
#pragma unroll
        for (int t = 0; t < 5; t++) acc[a][t] = mma16(fb[slot][t], fa[slot][a], acc[a][t]);
    }
    SB();
    if (i + K3_DEPTH < 19) load(i + K3_DEPTH, slot);
    SB();
  }
  float* rw = red + w * (64 * 84);
#pragma unroll
  for (int a = 0; a < 4; a++)
#pragma unroll
    for (int t = 0; t < 5; t++) *(f32x4*)(rw + (a * 16 + (l & 15)) * 84 + t * 16 + (l >> 4) * 4) = acc[a][t];
  __syncthreads();
#pragma unroll
  for (int i = 0; i < 5; i++) {
    const float* rp = red + er * 84 + ec + 4 * i;
    f32x4 v = *(const f32x4*)rp + *(const f32x4*)(rp + 64 * 84) + *(const f32x4*)(rp + 2 * 64 * 84) + *(const f32x4*)(rp + 3 * 64 * 84);
    u32x2 o = {pk2(v[0] + lo16(xr[i][0]), v[1] + hi16(xr[i][0])), pk2(v[2] + lo16(xr[i][1]), v[3] + hi16(xr[i][1]))};
    *(u32x2*)(out + eo + 4 * i) = o;
  }
}

extern "C" void kernel_launch(void* const* d_in, const int* in_sizes, int n_in, void* d_out, int out_size, void* d_ws, size_t ws_size,
                              hipStream_t stream) {
  const u16* x = (const u16*)d_in[0];
  char* ws = (char*)d_ws;
  k1_ln_kv<<<256, 512, 0, stream>>>(x, (const u16*)d_in[2], (const u16*)d_in[3], (const u16*)d_in[1], ws);
  k2_mix<<<N_ATT + N_GU + N_PACK, 512, 0, stream>>>((const u16*)d_in[6], (const u16*)d_in[7], (const u16*)d_in[4], (const u16*)d_in[8], ws);
  k3_out<<<256, 256, 0, stream>>>(x, ws, (u16*)d_out);
}
```

```cpp
#include <hip/hip_runtime.h>
#include <hip/hip_bf16.h>
#include <stdint.h>

typedef __attribute__((ext_vector_type(8))) __bf16 bf16x8;
typedef __attribute__((ext_vector_type(2))) __bf16 bf16x2;
typedef __attribute__((ext_vector_type(4))) short s16x4;
typedef __attribute__((ext_vector_type(16))) float f32x16;
typedef __attribute__((ext_vector_type(4))) float f32x4;
typedef __attribute__((ext_vector_type(2))) float f32x2;
typedef __attribute__((ext_vector_type(4))) unsigned u32x4;
typedef __attribute__((ext_vector_type(2))) unsigned u32x2;
#define LAS __attribute__((address_space(3)))
typedef unsigned short u16;

constexpr int S_ = 2048, D_ = 640, F_ = 1728;
constexpr int KS = 74;
constexpr size_t OFF_XN = 0;
constexpr size_t OFF_Q = OFF_XN + 2621440;
constexpr size_t OFF_KV = OFF_Q + 2621440;
constexpr size_t OFF_A = OFF_KV + 196608;
constexpr size_t OFF_W = OFF_A + 9699328;

__device__ __forceinline__ void glds16(const void* g, unsigned lds) {
  unsigned keep;
  asm volatile("s_mov_b32 %0, m0\n\ts_mov_b32 m0, %2\n\ts_nop 0\n\tglobal_load_lds_dwordx4 %1, off\n\ts_mov_b32 m0, %0"
               : "=&s"(keep) : "v"(g), "s"(lds) : "memory");
}
__device__ __forceinline__ unsigned pk2(float a, float b) {
  bf16x2 v = {(__bf16)a, (__bf16)b};
  return __builtin_bit_cast(unsigned, v);
}
__device__ __forceinline__ float bfr(float a) { return (float)(__bf16)a; }
__device__ __forceinline__ float lo16(unsigned u) { return __uint_as_float(u << 16); }
__device__ __forceinline__ float hi16(unsigned u) { return __uint_as_float(u & 0xffff0000u); }
__device__ __forceinline__ bf16x8 ldsr128(unsigned a) { return *(const LAS bf16x8*)(uintptr_t)a; }
__device__ __forceinline__ u32x2 ldsr64(unsigned a) { return *(const LAS u32x2*)(uintptr_t)a; }
__device__ __forceinline__ bf16x8 ldstr(unsigned a0, unsigned a1) {
  s16x4 x = __builtin_amdgcn_ds_read_tr16_b64_v4i16((LAS s16x4*)(uintptr_t)a0);
  s16x4 y = __builtin_amdgcn_ds_read_tr16_b64_v4i16((LAS s16x4*)(uintptr_t)a1);
  u32x2 xa = __builtin_bit_cast(u32x2, x), ya = __builtin_bit_cast(u32x2, y);
  u32x4 r = {xa[0], xa[1], ya[0], ya[1]};
  return __builtin_bit_cast(bf16x8, r);
}
__device__ __forceinline__ f32x16 mma32(bf16x8 a, bf16x8 b, f32x16 c) { return __builtin_amdgcn_mfma_f32_32x32x16_bf16(a, b, c, 0, 0, 0); }
__device__ __forceinline__ f32x4 mma16(bf16x8 a, bf16x8 b, f32x4 c) { return __builtin_amdgcn_mfma_f32_16x16x32_bf16(a, b, c, 0, 0, 0); }
__device__ __forceinline__ bf16x8 asbf(u32x4 v) { return __builtin_bit_cast(bf16x8, v); }
__device__ __forceinline__ float xhalf_max(float v) {
  auto r = __builtin_amdgcn_permlane32_swap(__float_as_uint(v), __float_as_uint(v), false, false);
  return fmaxf(__uint_as_float(r[0]), __uint_as_float(r[1]));
}
#define WAITV(n) asm volatile("s_waitcnt vmcnt(" #n ")" ::: "memory")
#define BAR() asm volatile("s_waitcnt lgkmcnt(0)\n\ts_barrier" ::: "memory")

#define SB() __builtin_amdgcn_sched_barrier(0)
__device__ __forceinline__ float row16_sum(float v) {
  v += __builtin_bit_cast(float, __builtin_amdgcn_update_dpp(0, __builtin_bit_cast(int, v), 0x128, 0xf, 0xf, false));
  v += __builtin_bit_cast(float, __builtin_amdgcn_update_dpp(0, __builtin_bit_cast(int, v), 0x124, 0xf, 0xf, false));
  v += __builtin_bit_cast(float, __builtin_amdgcn_update_dpp(0, __builtin_bit_cast(int, v), 0x122, 0xf, 0xf, false));
  v += __builtin_bit_cast(float, __builtin_amdgcn_update_dpp(0, __builtin_bit_cast(int, v), 0x121, 0xf, 0xf, false));
  return v;
}
__device__ __forceinline__ void pack_unit(int u, const u16* __restrict__ wo, const u16* __restrict__ wd, const u16* __restrict__ wq,
                                          char* __restrict__ ws) {
  const int ko = u / 80, no = u - ko * 80;
  const u16* src = (ko < 80 ? wo + (size_t)ko * 8 * D_ : (ko < 296 ? wd + (size_t)(ko - 80) * 8 * D_ : wq + (size_t)(ko - 296) * 8 * D_)) + no * 8;
  u32x4 r[8];
#pragma unroll
  for (int i = 0; i < 8; i++) r[i] = *(const u32x4*)(src + i * D_);
  char* dst = ko < 296 ? ws + OFF_W + (size_t)(no >> 1) * (KS * 1024) + ko * 256 + (no & 1) * 128
                       : ws + OFF_Q + (no >> 2) * 40960 + (ko - 296) * 512 + (no & 3) * 128;
#pragma unroll
  for (int j = 0; j < 4; j++) {
    u32x4 e, o;
#pragma unroll
    for (int i = 0; i < 4; i++) {
      e[i] = __builtin_amdgcn_perm(r[2 * i + 1][j], r[2 * i][j], 0x05040100u);
      o[i] = __builtin_amdgcn_perm(r[2 * i + 1][j], r[2 * i][j], 0x07060302u);
    }
    *(u32x4*)(dst + j * 32) = e;
    *(u32x4*)(dst + j * 32 + 16) = o;
  }
}
constexpr int K1_KV = 40960, K1_RED = K1_KV + 25600 + 128, K1_LDS = K1_RED + 3 * 4096;
__global__ void __launch_bounds__(512, 2) k1_ln_kv(const u16* __restrict__ x, const u16* __restrict__ wk, const u16* __restrict__ wv,
                                                   const u16* __restrict__ wq, char* __restrict__ ws) {
  __shared__ __attribute__((aligned(16))) char smem[K1_LDS];
  const unsigned sb = (unsigned)(uintptr_t)(LAS char*)smem;
  const int tid = threadIdx.x, l = tid & 63, w = __builtin_amdgcn_readfirstlane(tid >> 6);
  const int b = blockIdx.x;
  if (b >= 128) {
    if (tid < 50) pack_unit(296 * 80 + (b - 128) * 50 + tid, wq, wq, wq, ws);
    return;
  }
  const int cg = b & 1, rb = b >> 1, r = tid >> 4, p = tid & 15;
  u32x4 xv[5];
  const u16* xp = x + (size_t)(rb * 32 + r) * D_ + p * 8;
#pragma unroll
  for (int i = 0; i < 5; i++) xv[i] = *(const u32x4*)(xp + i * 128);
  SB();
  {
    const u16* src = (cg == 0 ? wk : wv) + l * 8;
#pragma unroll
    for (int j = 0; j < 3; j++) glds16(src + (w * 3 + j) * 512, sb + K1_KV + (w * 3 + j) * 1024);
    if (w == 7) glds16(src + 24 * 512, sb + K1_KV + 24 * 1024);
  }
  float s = 0.f, s1 = 0.f;
  const bf16x2 one2 = {(__bf16)1.f, (__bf16)1.f};
#pragma unroll
  for (int i = 0; i < 5; i++)
#pragma unroll
    for (int j = 0; j < 4; j += 2) {
      const unsigned u0 = xv[i][j], u1 = xv[i][j + 1];
      s = __builtin_amdgcn_fdot2_f32_bf16(__builtin_bit_cast(bf16x2, u0), one2, s, false);
      s1 = __builtin_amdgcn_fdot2_f32_bf16(__builtin_bit_cast(bf16x2, u1), one2, s1, false);
    }
  const float mean = row16_sum(s + s1) * (1.f / D_), mb = bfr(mean);
  f32x2 v[20], qa = {0.f, 0.f}, qb = {0.f, 0.f};
#pragma unroll
  for (int i = 0; i < 20; i++) {
    v[i] = f32x2{lo16(xv[i >> 2][i & 3]), hi16(xv[i >> 2][i & 3])} - f32x2{mb, mb};
    if (i & 1) qb += v[i] * v[i]; else qa += v[i] * v[i];
  }
  qa += qb;
  const float dm = mean - mb, vb = bfr(fmaxf(row16_sum(qa[0] + qa[1]) * (1.f / D_) - dm * dm, 0.f));
  const float rs = __builtin_amdgcn_rcpf(bfr(__builtin_amdgcn_sqrtf(bfr(vb + 1e-5f))));
  char* xnf = ws + OFF_XN + (size_t)rb * 40960;
  WAITV(0);
#pragma unroll
  for (int i = 0; i < 5; i++) {
    u32x4 o;
#pragma unroll
    for (int j = 0; j < 4; j++) {
      const unsigned d = pk2(v[i * 4 + j][0], v[i * 4 + j][1]);
      const f32x2 t = f32x2{lo16(d), hi16(d)} * f32x2{rs, rs};
      o[j] = pk2(t[0], t[1]);
    }
    const int c = p + 16 * i;
    *(LAS u32x4*)(uintptr_t)(sb + c * 512 + ((r ^ (c & 7)) << 4)) = o;
    if (i == 4 ? ((p >> 3) == cg) : ((i >> 1) == cg)) *(u32x4*)(xnf + c * 512 + r * 16) = o;
  }
  BAR();
  const int hh = l >> 5, m = l & 31;
  f32x16 acc = {};
  if (w < 4) {
    const unsigned ta = sb + K1_KV + (hh * 8 + ((l & 15) >> 2)) * 40 + ((l >> 4) & 1) * 32 + (l & 3) * 8 + w * 6400;
    bf16x8 xf[10], wf[10];
#pragma unroll
    for (int k = 0; k < 10; k++) {
      const int c = (w * 10 + k) * 2 + hh;
      xf[k] = ldsr128(sb + c * 512 + ((m ^ (c & 7)) << 4));
      wf[k] = ldstr(ta + k * 640, ta + k * 640 + 160);
    }
    if (cg == 0) {
#pragma unroll
      for (int k = 0; k < 10; k++) acc = mma32(wf[k], xf[k], acc);
    } else {
#pragma unroll
      for (int k = 0; k < 10; k++) acc = mma32(xf[k], wf[k], acc);
    }
    if (w > 0) {
#pragma unroll
      for (int g = 0; g < 4; g++)
        *(LAS f32x4*)(uintptr_t)(sb + K1_RED + (w - 1) * 4096 + g * 1024 + l * 16) = f32x4{acc[4 * g], acc[4 * g + 1], acc[4 * g + 2], acc[4 * g + 3]};
    }
  }
  BAR();
  if (w == 0) {
#pragma unroll
    for (int q = 0; q < 3; q++)
#pragma unroll
      for (int g = 0; g < 4; g++) {
        f32x4 t = *(const LAS f32x4*)(uintptr_t)(sb + K1_RED + q * 4096 + g * 1024 + l * 16);
#pragma unroll
        for (int i = 0; i < 4; i++) acc[4 * g + i] += t[i];
      }
    char* tile = ws + OFF_KV + (size_t)rb * 3072;
    if (cg == 0) {
      u32x4 o = {pk2(acc[0], acc[1]), pk2(acc[2], acc[3]), pk2(acc[4], acc[5]), pk2(acc[6], acc[7])};
      *(u32x4*)(tile + l * 16) = o;
      u32x2 o2 = {hh ? 0x3f803f80u : pk2(acc[8], acc[9]), hh ? 0u : pk2(acc[10], acc[11])};
      *(u32x2*)(tile + 1024 + l * 8) = o2;
    } else if (m < 24) {
#pragma unroll
      for (int s2 = 0; s2 < 2; s2++) {
        u32x4 o = {pk2(acc[8 * s2], acc[8 * s2 + 1]), pk2(acc[8 * s2 + 2], acc[8 * s2 + 3]), pk2(acc[8 * s2 + 4], acc[8 * s2 + 5]), pk2(acc[8 * s2 + 6], acc[8 * s2 + 7])};
        if (m >= 20) o = u32x4{0x3f803f80u, 0x3f803f80u, 0x3f803f80u, 0x3f803f80u};
        *(u32x4*)(tile + 1536 + (s2 * 2 + hh) * 384 + m * 16) = o;
      }
    }
  }
}

constexpr int K2_LDS = 3 * 24576 + 2048;
constexpr int QD = 20;
constexpr int N_ATT = 256, N_GU = 216, N_PACK = 40;

__device__ __forceinline__ void packP(const f32x16& s, bf16x8& p0, bf16x8& p1) {
  u32x4 a, b;
#pragma unroll
  for (int j = 0; j < 4; j++) {
    a[j] = pk2(__builtin_amdgcn_exp2f(s[2 * j]), __builtin_amdgcn_exp2f(s[2 * j + 1]));
    b[j] = pk2(__builtin_amdgcn_exp2f(s[8 + 2 * j]), __builtin_amdgcn_exp2f(s[8 + 2 * j + 1]));
  }
  p0 = asbf(a); p1 = asbf(b);
}
__device__ __forceinline__ float max16(const f32x16& s) {
  float a = fmaxf(fmaxf(s[0], s[1]), fmaxf(s[2], s[3]));
#pragma unroll
  for (int j = 4; j < 16; j += 2) a = fmaxf(a, fmaxf(s[j], s[j + 1]));
  return a;
}
__device__ __forceinline__ float split_ref(float mv, unsigned& slot) {
  float hi = bfr(mv), lo = bfr(mv - hi);
  slot = pk2(-hi, -lo);
  return hi + lo;
}

__device__ __forceinline__ void attn_body(int a, char* __restrict__ ws, unsigned sb) {
  const int tid = threadIdx.x, l = tid & 63, w = __builtin_amdgcn_readfirstlane(tid >> 6);
  const int hh = l >> 5, m = l & 31;
  const int qb = (a & 1) * 32 + (a >> 3), hg = (a >> 1) & 3, head = hg * 8 + w;
  __builtin_amdgcn_s_setprio(3);
  const char* kvg = ws + OFF_KV;
  const char* src = kvg + w * 3072 + l * 16;
  const int row = qb * 32 + m;
  {
    const char* wp = ws + OFF_Q + (hg * 5 + (w < 5 ? w : 4)) * 40960 + l * 16;
    bf16x8 wf[QD], xf[2][2];
    if (w < 5) {
#pragma unroll
      for (int k = 0; k < QD; k++) wf[k] = *(const bf16x8*)(wp + k * 1024);
    }
    SB();
#pragma unroll
    for (int j = 0; j < 5; j++) glds16(ws + OFF_XN + (size_t)qb * 40960 + (w * 5 + j) * 1024 + l * 16, sb + (w * 5 + j) * 1024);
#pragma unroll
    for (int j = 0; j < 3; j++) glds16(src + j * 1024, sb + 49152 + w * 3072 + j * 1024);
    WAITV(3); BAR();
    if (w < 5) {
      f32x16 acc = {};
#pragma unroll
      for (int i = 0; i < 2; i++) xf[0][i] = ldsr128(sb + i * 1024 + l * 16);
#pragma unroll
      for (int g = 0; g < 20; g++) {
        SB();
        if (g < 19) {
#pragma unroll
          for (int i = 0; i < 2; i++) xf[(g + 1) & 1][i] = ldsr128(sb + (g * 2 + 2 + i) * 1024 + l * 16);
        }
        SB();
#pragma unroll
        for (int i = 0; i < 2; i++) {
          const int k = g * 2 + i;
          acc = mma32(wf[k % QD], xf[g & 1][i], acc);
          SB();
          if (k + QD < 40) wf[k % QD] = *(const bf16x8*)(wp + (k + QD) * 1024);
        }
      }
      const float sc = 0.22360679774997896f * 1.4426950408889634f;
      const unsigned qw = sb + (w < 4 ? 40960 + w * 2048 : 73728) + hh * 256 + m * 8;
#pragma unroll
      for (int g = 0; g < 4; g++)
        *(LAS u32x2*)(uintptr_t)(qw + g * 512) = u32x2{pk2(bfr(acc[4 * g]) * sc, bfr(acc[4 * g + 1]) * sc), pk2(bfr(acc[4 * g + 2]) * sc, bfr(acc[4 * g + 3]) * sc)};
    }
  }
  unsigned cur = sb + 49152, nxt = sb, fre = sb + 24576;
  const unsigned ka = l * 16, kb = 1024 + l * 8, va = 1536 + hh * 384 + (m < 20 ? m : 20) * 16;
  WAITV(0); BAR();
#pragma unroll
  for (int j = 0; j < 3; j++) glds16(src + 24576 + j * 1024, sb + w * 3072 + j * 1024);
  auto qrd = [&](int qd) { return ldsr64(sb + (qd < 32 ? 40960 : 73728 - 8192) + qd * 256 + m * 8); };
  const u32x2 qa = qrd(w * 5 + hh), qc = qrd(w * 5 + 2 + hh), qd = qrd(w * 5 + 4);
  const bf16x8 q1 = asbf(u32x4{qa[0], qa[1], qc[0], qc[1]});
  u32x4 q2 = {hh ? 0u : qd[0], hh ? 0u : qd[1], 0u, 0u};
#define EX(v) __builtin_amdgcn_exp2f(v)
#define TR2(a, b) __builtin_amdgcn_perm(__float_as_uint(b), __float_as_uint(a), 0x07060302u)
  const f32x16 zero = {};
  u32x4 pb = {0u, 0u, 0u, 0u};
  float e0 = 0.f, e1 = 0.f, e2 = 0.f, e3 = 0.f;
  __builtin_amdgcn_s_setprio(2);
  bf16x8 k1 = ldsr128(cur + ka), v0 = ldsr128(cur + va), v1 = ldsr128(cur + va + 768);
  u32x4 k2 = {0u, 0u, 0u, 0u};
  { u32x2 t2 = ldsr64(cur + kb); k2[0] = t2[0]; k2[1] = t2[1]; }
  f32x16 s = mma32(asbf(k2), asbf(q2), mma32(k1, q1, zero)), o = {};
  k1 = ldsr128(cur + 3072 + ka);
  { u32x2 t2 = ldsr64(cur + 3072 + kb); k2[0] = t2[0]; k2[1] = t2[1]; }
  unsigned slot;
  float mref = split_ref(xhalf_max(max16(s)) + 60.f, slot);
  if (hh) q2[0] = slot;
#pragma unroll
  for (int j = 0; j < 16; j++) s[j] -= mref;
  for (int c = 0; c < 8; c++) {
    const unsigned ck = cur + ka, ck2 = cur + kb, cv = cur + va, nk = nxt + ka, nk2 = nxt + kb, nv = nxt + va;
#pragma unroll
    for (int t = 0; t < 8; t++) {
      if (t == 6) {
        WAITV(0); BAR();
        if (c < 6) {
#pragma unroll
          for (int j = 0; j < 3; j++) glds16(src + (c + 2) * 24576 + j * 1024, __builtin_amdgcn_readfirstlane(fre) + w * 3072 + j * 1024);
        }
      }
      if (t == 3) {
        if (__builtin_expect(__any(o[12] > 1024.f), 0)) {
          pb[2] = TR2(e0, e1); pb[3] = TR2(e2, e3);
          o = mma32(v1, asbf(pb), o);
          pb = u32x4{0u, 0u, 0u, 0u}; e0 = e1 = e2 = e3 = 0.f;
          float lm = xhalf_max(o[12]);
          int e = __builtin_amdgcn_frexp_expf(lm) + 59;
          float mn = split_ref(mref + (float)(e > 0 ? (e < 100 ? e : 100) : 0), slot);
          float dl = mn - mref, f = EX(-dl);
          mref = mn;
          if (hh) q2[0] = slot;
#pragma unroll
          for (int j = 0; j < 16; j++) { o[j] *= f; s[j] -= dl; }
        }
      }
      u32x4 pa;
#define SLOT(i, P0, P1) { float n0 = EX(s[i]), n1 = EX(s[i + 1]); SB(); P0 = TR2(e0, e1); SB(); float n2 = EX(s[i + 2]); SB(); \
                          P1 = TR2(e2, e3); SB(); e3 = EX(s[i + 3]); e0 = n0; e1 = n1; e2 = n2; SB(); }
      SB();
      f32x16 sn = mma32(k1, q1, zero);
      SB();
      k1 = ldsr128(t < 6 ? ck + (t + 2) * 3072 : nk + (t - 6) * 3072);
      SLOT(0, pb[2], pb[3])
      sn = mma32(asbf(k2), asbf(q2), sn);
      SB();
      { u32x2 t2 = ldsr64(t < 6 ? ck2 + (t + 2) * 3072 : nk2 + (t - 6) * 3072); k2[0] = t2[0]; k2[1] = t2[1]; }
      SLOT(4, pa[0], pa[1])
      o = mma32(v1, asbf(pb), o);
      SB();
      v1 = ldsr128(cv + t * 3072 + 768);
      SLOT(8, pa[2], pa[3])
      o = mma32(v0, asbf(pa), o);
      SB();
      v0 = ldsr128(t < 7 ? cv + (t + 1) * 3072 : nv);
      SLOT(12, pb[0], pb[1])
      s = sn;
    }
    unsigned tmp = cur; cur = nxt; nxt = fre; fre = tmp;
  }
  pb[2] = TR2(e0, e1); pb[3] = TR2(e2, e3);
  o = mma32(v1, asbf(pb), o);
  float chk = o[12];
#pragma unroll
  for (int j = 0; j < 12; j++) chk = fmaxf(chk, fabsf((hh && j >= 8) ? 0.f : o[j]) );
  bool bad = !(chk < 1e37f) || !(o[12] > 0.f);
#pragma unroll
  for (int j = 0; j < 13; j++) bad = bad || ((hh && j >= 8 && j < 12) ? false : (o[j] != o[j]));
  if (__builtin_expect(__any(bad), 0)) {
    if (hh) q2[0] = 0u;
    float mx = -INFINITY;
    o = f32x16{};
    for (int t = 0; t < 64; t++) {
      const char* tb = kvg + t * 3072;
      u32x2 k2 = *(const u32x2*)(tb + kb);
      f32x16 z = {};
      z = mma32(*(const bf16x8*)(tb + ka), q1, z);
      z = mma32(asbf(u32x4{k2[0], k2[1], 0u, 0u}), asbf(q2), z);
      float mn = fmaxf(mx, xhalf_max(max16(z)));
      float f = __builtin_amdgcn_exp2f(mx - mn);
      mx = mn;
#pragma unroll
      for (int j = 0; j < 16; j++) { o[j] *= f; z[j] -= mn; }
      bf16x8 p0, p1;
      packP(z, p0, p1);
      o = mma32(*(const bf16x8*)(tb + va), p0, o);
      o = mma32(*(const bf16x8*)(tb + va + 768), p1, o);
    }
  }
  const float inv = 1.f / o[12];
  char* ab = ws + OFF_A + (size_t)(row >> 4) * (KS * 1024) + (row & 15) * 16;
#pragma unroll
  for (int g = 0; g < 3; g++) {
    if (g < 2 || !hh) {
      const int k = head * 20 + 8 * g + 4 * hh;
      u32x2 ov = {pk2(o[4 * g] * inv, o[4 * g + 1] * inv), pk2(o[4 * g + 2] * inv, o[4 * g + 3] * inv)};
      *(u32x2*)(ab + (k >> 3) * 256 + ((k >> 2) & 1) * 8) = ov;
    }
  }
}

__device__ __forceinline__ void gu_body(int id, const u16* __restrict__ wg, const u16* __restrict__ wu, char* __restrict__ ws, unsigned sb) {
  const int tid = threadIdx.x, l = tid & 63, w = __builtin_amdgcn_readfirstlane(tid >> 6);
  const int hh = l >> 5, m = l & 31;
  const int T = ((id >> 1) & 3) * 27 + (id >> 3), nt = T >> 2, mt = (id & 1) * 4 + (T & 3);
  const char* xs = ws + OFF_XN + (size_t)(mt * 8 + w) * 40960 + l * 16;
  const u16* wsrc = ((w & 1) ? wu : wg) + (size_t)((w >> 1) * 8 + ((l & 31) >> 2)) * F_ + nt * 64 + (l >> 5) * 32 + (l & 3) * 8;
  auto issue = [&](int st, unsigned buf) {
    glds16(xs + st * 2048, buf + w * 2048);
    glds16(xs + st * 2048 + 1024, buf + w * 2048 + 1024);
    glds16(wsrc + (size_t)st * 32 * F_, buf + 16384 + w * 1024);
  };
  issue(0, sb); issue(1, sb + 24576);
  const int wm = w & 3, wn = w >> 2;
  const unsigned xa = wm * 4096 + l * 16;
  const unsigned wa = 16384 + hh * 2048 + wn * 512 + ((l & 15) >> 2) * 64 + ((l >> 4) & 1) * 32 + (l & 3) * 8;
  f32x16 acc[2][2] = {};
  unsigned cur = sb, nxt = sb + 24576, fre = sb + 49152;
  for (int st = 0; st < 20; st++) {
    if (st < 19) WAITV(3); else WAITV(0);
    BAR();
    if (st < 18) issue(st + 2, __builtin_amdgcn_readfirstlane(fre));
#pragma unroll
    for (int s = 0; s < 2; s++) {
      bf16x8 x0 = ldsr128(cur + xa + s * 1024), x1 = ldsr128(cur + xa + 2048 + s * 1024);
      bf16x8 g = ldstr(cur + wa + s * 4096, cur + wa + s * 4096 + 256);
      bf16x8 u = ldstr(cur + wa + s * 4096 + 1024, cur + wa + s * 4096 + 1024 + 256);
      acc[0][0] = mma32(g, x0, acc[0][0]); acc[0][1] = mma32(g, x1, acc[0][1]);
      acc[1][0] = mma32(u, x0, acc[1][0]); acc[1][1] = mma32(u, x1, acc[1][1]);
    }
    unsigned tmp = cur; cur = nxt; nxt = fre; fre = tmp;
  }
#pragma unroll
  for (int mb = 0; mb < 2; mb++) {
    const int row = mt * 256 + wm * 64 + mb * 32 + m;
    char* ab = ws + OFF_A + (size_t)(row >> 4) * (KS * 1024) + (20 + nt * 2 + wn) * 1024 + (row & 15) * 16 + hh * 8;
#pragma unroll
    for (int g = 0; g < 4; g++) {
      float h[4];
#pragma unroll
      for (int i = 0; i < 4; i++) {
        float gv = acc[0][mb][4 * g + i], uv = acc[1][mb][4 * g + i];
        h[i] = gv * __builtin_amdgcn_rcpf(1.f + __builtin_amdgcn_exp2f(gv * -1.4426950408889634f)) * uv;
      }
      *(u32x2*)(ab + g * 256) = u32x2{pk2(h[0], h[1]), pk2(h[2], h[3])};
    }
  }
}

#define GPRIO 0
#define GDEL 0
__global__ void __launch_bounds__(512, 4) k2_mix(const u16* __restrict__ wg, const u16* __restrict__ wu, const u16* __restrict__ wo,
                                                 const u16* __restrict__ wd, char* __restrict__ ws) {
  __shared__ __attribute__((aligned(16))) char smem[K2_LDS];
  const unsigned sb = (unsigned)(uintptr_t)(LAS char*)smem;
  const int b = blockIdx.x;
  if (b < N_ATT) attn_body(b, ws, sb);
  else if (b < N_ATT + N_GU) {
    __builtin_amdgcn_s_setprio(GPRIO);
    if (GDEL) __builtin_amdgcn_s_sleep(GDEL);
    gu_body(b - N_ATT, wg, wu, ws, sb);
  } else {
    __builtin_amdgcn_s_sleep(127); __builtin_amdgcn_s_sleep(64);
    for (int u = (b - N_ATT - N_GU) * 512 + threadIdx.x; u < 296 * 80; u += N_PACK * 512) pack_unit(u, wo, wd, wd, ws);
  }
}

constexpr int K3_DEPTH = 5;
__global__ void __launch_bounds__(256, 1) k3_out(const u16* __restrict__ x, const char* __restrict__ ws, u16* __restrict__ out) {
  __shared__ __attribute__((aligned(16))) float red[4 * 64 * 84];
  const int tid = threadIdx.x, l = tid & 63, w = __builtin_amdgcn_readfirstlane(tid >> 6);
  const int b = blockIdx.x, xcd = b & 7, j = b >> 3;
  const int mt = (xcd >> 1) * 8 + (j & 7), nt = (xcd & 1) * 4 + (j >> 3);
  const char* ap = ws + OFF_A + (size_t)mt * 4 * (KS * 1024) + l * 16;
  const char* bp = ws + OFF_W + (size_t)nt * 5 * (KS * 1024) + l * 16;
  bf16x8 fa[K3_DEPTH][4], fb[K3_DEPTH][5];
  f32x4 acc[4][5] = {};
  auto load = [&](int i, int slot) {
    int ks = w + 4 * i; ks = ks < KS ? ks : KS - 1;
#pragma unroll
    for (int t = 0; t < 5; t++) fb[slot][t] = *(const bf16x8*)(bp + (size_t)(t * KS + ks) * 1024);
#pragma unroll
    for (int t = 0; t < 4; t++) fa[slot][t] = *(const bf16x8*)(ap + (size_t)(t * KS + ks) * 1024);
  };
#pragma unroll
  for (int i = 0; i < K3_DEPTH; i++) { load(i, i); SB(); }
  const int er = tid >> 2, ec = (tid & 3) * 20;
  const size_t eo = (size_t)(mt * 64 + er) * D_ + nt * 80 + ec;
  u32x2 xr[5];
#pragma unroll
  for (int i = 0; i < 5; i++) xr[i] = *(const u32x2*)(x + eo + 4 * i);
  SB();
#pragma unroll
  for (int i = 0; i < 19; i++) {
    const int slot = i % K3_DEPTH;
    if (i < 18 || w < 2) {
#pragma unroll
      for (int a = 0; a < 4; a++)
#pragma unroll
        for (int t = 0; t < 5; t++) acc[a][t] = mma16(fb[slot][t], fa[slot][a], acc[a][t]);
    }
    SB();
    if (i + K3_DEPTH < 19) load(i + K3_DEPTH, slot);
    SB();
  }
  float* rw = red + w * (64 * 84);
#pragma unroll
  for (int a = 0; a < 4; a++)
#pragma unroll
    for (int t = 0; t < 5; t++) *(f32x4*)(rw + (a * 16 + (l & 15)) * 84 + t * 16 + (l >> 4) * 4) = acc[a][t];
  __syncthreads();
#pragma unroll
  for (int i = 0; i < 5; i++) {
    const float* rp = red + er * 84 + ec + 4 * i;
    f32x4 v = *(const f32x4*)rp + *(const f32x4*)(rp + 64 * 84) + *(const f32x4*)(rp + 2 * 64 * 84) + *(const f32x4*)(rp + 3 * 64 * 84);
    u32x2 o = {pk2(v[0] + lo16(xr[i][0]), v[1] + hi16(xr[i][0])), pk2(v[2] + lo16(xr[i][1]), v[3] + hi16(xr[i][1]))};
    *(u32x2*)(out + eo + 4 * i) = o;
  }
}

extern "C" void kernel_launch(void* const* d_in, const int* in_sizes, int n_in, void* d_out, int out_size, void* d_ws, size_t ws_size,
                              hipStream_t stream) {
  const u16* x = (const u16*)d_in[0];
  char* ws = (char*)d_ws;
  k1_ln_kv<<<256, 512, 0, stream>>>(x, (const u16*)d_in[2], (const u16*)d_in[3], (const u16*)d_in[1], ws);
  k2_mix<<<N_ATT + N_GU + N_PACK, 512, 0, stream>>>((const u16*)d_in[6], (const u16*)d_in[7], (const u16*)d_in[4], (const u16*)d_in[8], ws);
  k3_out<<<256, 256, 0, stream>>>(x, ws, (u16*)d_out);
}
```

```cpp
#include <hip/hip_runtime.h>
#include <hip/hip_bf16.h>
#include <stdint.h>

typedef __attribute__((ext_vector_type(8))) __bf16 bf16x8;
typedef __attribute__((ext_vector_type(2))) __bf16 bf16x2;
typedef __attribute__((ext_vector_type(4))) short s16x4;
typedef __attribute__((ext_vector_type(16))) float f32x16;
typedef __attribute__((ext_vector_type(4))) float f32x4;
typedef __attribute__((ext_vector_type(2))) float f32x2;
typedef __attribute__((ext_vector_type(4))) unsigned u32x4;
typedef __attribute__((ext_vector_type(2))) unsigned u32x2;
#define LAS __attribute__((address_space(3)))
typedef unsigned short u16;

constexpr int S_ = 2048, D_ = 640, F_ = 1728;
constexpr int KS = 74;
constexpr size_t OFF_XN = 0;
constexpr size_t OFF_Q = OFF_XN + 2621440;
constexpr size_t OFF_KV = OFF_Q + 2621440;
constexpr size_t OFF_A = OFF_KV + 196608;
constexpr size_t OFF_W = OFF_A + 9699328;

__device__ __forceinline__ void glds16(const void* g, unsigned lds) {
  unsigned keep;
  asm volatile("s_mov_b32 %0, m0\n\ts_mov_b32 m0, %2\n\ts_nop 0\n\tglobal_load_lds_dwordx4 %1, off\n\ts_mov_b32 m0, %0"
               : "=&s"(keep) : "v"(g), "s"(lds) : "memory");
}
__device__ __forceinline__ void touch(const void* g, unsigned lds) {
  unsigned keep;
  asm volatile("s_mov_b32 %0, m0\n\ts_mov_b32 m0, %2\n\ts_nop 0\n\tglobal_load_lds_dword %1, off\n\ts_mov_b32 m0, %0"
               : "=&s"(keep) : "v"(g), "s"(lds) : "memory");
}
__device__ __forceinline__ unsigned pk2(float a, float b) {
  bf16x2 v = {(__bf16)a, (__bf16)b};
  return __builtin_bit_cast(unsigned, v);
}
__device__ __forceinline__ float bfr(float a) { return (float)(__bf16)a; }
__device__ __forceinline__ float lo16(unsigned u) { return __uint_as_float(u << 16); }
__device__ __forceinline__ float hi16(unsigned u) { return __uint_as_float(u & 0xffff0000u); }
__device__ __forceinline__ bf16x8 ldsr128(unsigned a) { return *(const LAS bf16x8*)(uintptr_t)a; }
__device__ __forceinline__ u32x2 ldsr64(unsigned a) { return *(const LAS u32x2*)(uintptr_t)a; }
__device__ __forceinline__ bf16x8 ldstr(unsigned a0, unsigned a1) {
  s16x4 x = __builtin_amdgcn_ds_read_tr16_b64_v4i16((LAS s16x4*)(uintptr_t)a0);
  s16x4 y = __builtin_amdgcn_ds_read_tr16_b64_v4i16((LAS s16x4*)(uintptr_t)a1);
  u32x2 xa = __builtin_bit_cast(u32x2, x), ya = __builtin_bit_cast(u32x2, y);
  u32x4 r = {xa[0], xa[1], ya[0], ya[1]};
  return __builtin_bit_cast(bf16x8, r);
}
__device__ __forceinline__ f32x16 mma32(bf16x8 a, bf16x8 b, f32x16 c) { return __builtin_amdgcn_mfma_f32_32x32x16_bf16(a, b, c, 0, 0, 0); }
__device__ __forceinline__ f32x4 mma16(bf16x8 a, bf16x8 b, f32x4 c) { return __builtin_amdgcn_mfma_f32_16x16x32_bf16(a, b, c, 0, 0, 0); }
__device__ __forceinline__ bf16x8 asbf(u32x4 v) { return __builtin_bit_cast(bf16x8, v); }
__device__ __forceinline__ float xhalf_max(float v) {
  auto r = __builtin_amdgcn_permlane32_swap(__float_as_uint(v), __float_as_uint(v), false, false);
  return fmaxf(__uint_as_float(r[0]), __uint_as_float(r[1]));
}
#define WAITV(n) asm volatile("s_waitcnt vmcnt(" #n ")" ::: "memory")
#define BAR() asm volatile("s_waitcnt lgkmcnt(0)\n\ts_barrier" ::: "memory")

#define SB() __builtin_amdgcn_sched_barrier(0)
__device__ __forceinline__ float row16_sum(float v) {
  v += __builtin_bit_cast(float, __builtin_amdgcn_update_dpp(0, __builtin_bit_cast(int, v), 0x128, 0xf, 0xf, false));
  v += __builtin_bit_cast(float, __builtin_amdgcn_update_dpp(0, __builtin_bit_cast(int, v), 0x124, 0xf, 0xf, false));
  v += __builtin_bit_cast(float, __builtin_amdgcn_update_dpp(0, __builtin_bit_cast(int, v), 0x122, 0xf, 0xf, false));
  v += __builtin_bit_cast(float, __builtin_amdgcn_update_dpp(0, __builtin_bit_cast(int, v), 0x121, 0xf, 0xf, false));
  return v;
}
__device__ __forceinline__ void pack_unit(int u, const u16* __restrict__ wo, const u16* __restrict__ wd, const u16* __restrict__ wq,
                                          char* __restrict__ ws) {
  const int ko = u / 80, no = u - ko * 80;
  const u16* src = (ko < 80 ? wo + (size_t)ko * 8 * D_ : (ko < 296 ? wd + (size_t)(ko - 80) * 8 * D_ : wq + (size_t)(ko - 296) * 8 * D_)) + no * 8;
  u32x4 r[8];
#pragma unroll
  for (int i = 0; i < 8; i++) r[i] = *(const u32x4*)(src + i * D_);
  char* dst = ko < 296 ? ws + OFF_W + (size_t)(no >> 1) * (KS * 1024) + ko * 256 + (no & 1) * 128
                       : ws + OFF_Q + (no >> 2) * 40960 + (ko - 296) * 512 + (no & 3) * 128;
#pragma unroll
  for (int j = 0; j < 4; j++) {
    u32x4 e, o;
#pragma unroll
    for (int i = 0; i < 4; i++) {
      e[i] = __builtin_amdgcn_perm(r[2 * i + 1][j], r[2 * i][j], 0x05040100u);
      o[i] = __builtin_amdgcn_perm(r[2 * i + 1][j], r[2 * i][j], 0x07060302u);
    }
    *(u32x4*)(dst + j * 32) = e;
    *(u32x4*)(dst + j * 32 + 16) = o;
  }
}
constexpr int K1_KV = 40960, K1_RED = K1_KV + 25600 + 128, K1_LDS = K1_RED + 3 * 4096;
__global__ void __launch_bounds__(512, 2) k1_ln_kv(const u16* __restrict__ x, const u16* __restrict__ wk, const u16* __restrict__ wv,
                                                   const u16* __restrict__ wq, const u16* __restrict__ wg, const u16* __restrict__ wu,
                                                   const u16* __restrict__ wd, const u16* __restrict__ wo, char* __restrict__ ws) {
  __shared__ __attribute__((aligned(16))) char smem[K1_LDS];
  const unsigned sb = (unsigned)(uintptr_t)(LAS char*)smem;
  const int tid = threadIdx.x, l = tid & 63, w = __builtin_amdgcn_readfirstlane(tid >> 6);
  const int b = blockIdx.x;
  if (b >= 128) {
    if (tid < 50) pack_unit(296 * 80 + (b - 128) * 50 + tid, wq, wq, wq, ws);
    if (w > 0) {
      const int ln = ((b - 128) * 7 + w - 1) * 64 + l, q = ln / 17280;
      touch((q == 0 ? wg : (q == 1 ? wu : (q == 2 ? wd : wo))) + (ln - q * 17280) * 64, sb);
      WAITV(0);
    }
    return;
  }
  const int cg = b & 1, rb = b >> 1, r = tid >> 4, p = tid & 15;
  u32x4 xv[5];
  const u16* xp = x + (size_t)(rb * 32 + r) * D_ + p * 8;
#pragma unroll
  for (int i = 0; i < 5; i++) xv[i] = *(const u32x4*)(xp + i * 128);
  SB();
  {
    const u16* src = (cg == 0 ? wk : wv) + l * 8;
#pragma unroll
    for (int j = 0; j < 3; j++) glds16(src + (w * 3 + j) * 512, sb + K1_KV + (w * 3 + j) * 1024);
    if (w == 7) glds16(src + 24 * 512, sb + K1_KV + 24 * 1024);
  }
  float s = 0.f, s1 = 0.f;
  const bf16x2 one2 = {(__bf16)1.f, (__bf16)1.f};
#pragma unroll
  for (int i = 0; i < 5; i++)
#pragma unroll
    for (int j = 0; j < 4; j += 2) {
      const unsigned u0 = xv[i][j], u1 = xv[i][j + 1];
      s = __builtin_amdgcn_fdot2_f32_bf16(__builtin_bit_cast(bf16x2, u0), one2, s, false);
      s1 = __builtin_amdgcn_fdot2_f32_bf16(__builtin_bit_cast(bf16x2, u1), one2, s1, false);
    }
  const float mean = row16_sum(s + s1) * (1.f / D_), mb = bfr(mean);
  f32x2 v[20], qa = {0.f, 0.f}, qb = {0.f, 0.f};
#pragma unroll
  for (int i = 0; i < 20; i++) {
    v[i] = f32x2{lo16(xv[i >> 2][i & 3]), hi16(xv[i >> 2][i & 3])} - f32x2{mb, mb};
    if (i & 1) qb += v[i] * v[i]; else qa += v[i] * v[i];
  }
  qa += qb;
  const float dm = mean - mb, vb = bfr(fmaxf(row16_sum(qa[0] + qa[1]) * (1.f / D_) - dm * dm, 0.f));
  const float rs = __builtin_amdgcn_rcpf(bfr(__builtin_amdgcn_sqrtf(bfr(vb + 1e-5f))));
  char* xnf = ws + OFF_XN + (size_t)rb * 40960;
  WAITV(0);
#pragma unroll
  for (int i = 0; i < 5; i++) {
    u32x4 o;
#pragma unroll
    for (int j = 0; j < 4; j++) {
      const unsigned d = pk2(v[i * 4 + j][0], v[i * 4 + j][1]);
      const f32x2 t = f32x2{lo16(d), hi16(d)} * f32x2{rs, rs};
      o[j] = pk2(t[0], t[1]);
    }
    const int c = p + 16 * i;
    *(LAS u32x4*)(uintptr_t)(sb + c * 512 + ((r ^ (c & 7)) << 4)) = o;
    if (i == 4 ? ((p >> 3) == cg) : ((i >> 1) == cg)) *(u32x4*)(xnf + c * 512 + r * 16) = o;
  }
  BAR();
  const int hh = l >> 5, m = l & 31;
  f32x16 acc = {};
  if (w < 4) {
    const unsigned ta = sb + K1_KV + (hh * 8 + ((l & 15) >> 2)) * 40 + ((l >> 4) & 1) * 32 + (l & 3) * 8 + w * 6400;
    bf16x8 xf[10], wf[10];
#pragma unroll
    for (int k = 0; k < 10; k++) {
      const int c = (w * 10 + k) * 2 + hh;
      xf[k] = ldsr128(sb + c * 512 + ((m ^ (c & 7)) << 4));
      wf[k] = ldstr(ta + k * 640, ta + k * 640 + 160);
    }
    if (cg == 0) {
#pragma unroll
      for (int k = 0; k < 10; k++) acc = mma32(wf[k], xf[k], acc);
    } else {
#pragma unroll
      for (int k = 0; k < 10; k++) acc = mma32(xf[k], wf[k], acc);
    }
    if (w > 0) {
#pragma unroll
      for (int g = 0; g < 4; g++)
        *(LAS f32x4*)(uintptr_t)(sb + K1_RED + (w - 1) * 4096 + g * 1024 + l * 16) = f32x4{acc[4 * g], acc[4 * g + 1], acc[4 * g + 2], acc[4 * g + 3]};
    }
  }
  BAR();
  if (w == 0) {
#pragma unroll
    for (int q = 0; q < 3; q++)
#pragma unroll
      for (int g = 0; g < 4; g++) {
        f32x4 t = *(const LAS f32x4*)(uintptr_t)(sb + K1_RED + q * 4096 + g * 1024 + l * 16);
#pragma unroll
        for (int i = 0; i < 4; i++) acc[4 * g + i] += t[i];
      }
    char* tile = ws + OFF_KV + (size_t)rb * 3072;
    if (cg == 0) {
      u32x4 o = {pk2(acc[0], acc[1]), pk2(acc[2], acc[3]), pk2(acc[4], acc[5]), pk2(acc[6], acc[7])};
      *(u32x4*)(tile + l * 16) = o;
      u32x2 o2 = {hh ? 0x3f803f80u : pk2(acc[8], acc[9]), hh ? 0u : pk2(acc[10], acc[11])};
      *(u32x2*)(tile + 1024 + l * 8) = o2;
    } else if (m < 24) {
#pragma unroll
      for (int s2 = 0; s2 < 2; s2++) {
        u32x4 o = {pk2(acc[8 * s2], acc[8 * s2 + 1]), pk2(acc[8 * s2 + 2], acc[8 * s2 + 3]), pk2(acc[8 * s2 + 4], acc[8 * s2 + 5]), pk2(acc[8 * s2 + 6], acc[8 * s2 + 7])};
        if (m >= 20) o = u32x4{0x3f803f80u, 0x3f803f80u, 0x3f803f80u, 0x3f803f80u};
        *(u32x4*)(tile + 1536 + (s2 * 2 + hh) * 384 + m * 16) = o;
      }
    }
  }
}

constexpr int K2_LDS = 3 * 24576 + 2048;
constexpr int QD = 20;
constexpr int N_ATT = 256, N_GU = 216, N_PACK = 40;

__device__ __forceinline__ void packP(const f32x16& s, bf16x8& p0, bf16x8& p1) {
  u32x4 a, b;
#pragma unroll
  for (int j = 0; j < 4; j++) {
    a[j] = pk2(__builtin_amdgcn_exp2f(s[2 * j]), __builtin_amdgcn_exp2f(s[2 * j + 1]));
    b[j] = pk2(__builtin_amdgcn_exp2f(s[8 + 2 * j]), __builtin_amdgcn_exp2f(s[8 + 2 * j + 1]));
  }
  p0 = asbf(a); p1 = asbf(b);
}
__device__ __forceinline__ float max16(const f32x16& s) {
  float a = fmaxf(fmaxf(s[0], s[1]), fmaxf(s[2], s[3]));
#pragma unroll
  for (int j = 4; j < 16; j += 2) a = fmaxf(a, fmaxf(s[j], s[j + 1]));
  return a;
}
__device__ __forceinline__ float split_ref(float mv, unsigned& slot) {
  float hi = bfr(mv), lo = bfr(mv - hi);
  slot = pk2(-hi, -lo);
  return hi + lo;
}

__device__ __forceinline__ void attn_body(int a, char* __restrict__ ws, unsigned sb) {
  const int tid = threadIdx.x, l = tid & 63, w = __builtin_amdgcn_readfirstlane(tid >> 6);
  const int hh = l >> 5, m = l & 31;
  const int qb = (a & 1) * 32 + (a >> 3), hg = (a >> 1) & 3, head = hg * 8 + w;
  __builtin_amdgcn_s_setprio(3);
  const char* kvg = ws + OFF_KV;
  const char* src = kvg + w * 3072 + l * 16;
  const int row = qb * 32 + m;
  {
    const char* wp = ws + OFF_Q + (hg * 5 + (w < 5 ? w : 4)) * 40960 + l * 16;
    bf16x8 wf[QD], xf[2][2];
    if (w < 5) {
#pragma unroll
      for (int k = 0; k < QD; k++) wf[k] = *(const bf16x8*)(wp + k * 1024);
    }
    SB();
#pragma unroll
    for (int j = 0; j < 5; j++) glds16(ws + OFF_XN + (size_t)qb * 40960 + (w * 5 + j) * 1024 + l * 16, sb + (w * 5 + j) * 1024);
#pragma unroll
    for (int j = 0; j < 3; j++) glds16(src + j * 1024, sb + 49152 + w * 3072 + j * 1024);
    WAITV(3); BAR();
    if (w < 5) {
      f32x16 acc = {};
#pragma unroll
      for (int i = 0; i < 2; i++) xf[0][i] = ldsr128(sb + i * 1024 + l * 16);
#pragma unroll
      for (int g = 0; g < 20; g++) {
        SB();
        if (g < 19) {
#pragma unroll
          for (int i = 0; i < 2; i++) xf[(g + 1) & 1][i] = ldsr128(sb + (g * 2 + 2 + i) * 1024 + l * 16);
        }
        SB();
#pragma unroll
        for (int i = 0; i < 2; i++) {
          const int k = g * 2 + i;
          acc = mma32(wf[k % QD], xf[g & 1][i], acc);
          SB();
          if (k + QD < 40) wf[k % QD] = *(const bf16x8*)(wp + (k + QD) * 1024);
        }
      }
      const float sc = 0.22360679774997896f * 1.4426950408889634f;
      const unsigned qw = sb + (w < 4 ? 40960 + w * 2048 : 73728) + hh * 256 + m * 8;
#pragma unroll
      for (int g = 0; g < 4; g++)
        *(LAS u32x2*)(uintptr_t)(qw + g * 512) = u32x2{pk2(bfr(acc[4 * g]) * sc, bfr(acc[4 * g + 1]) * sc), pk2(bfr(acc[4 * g + 2]) * sc, bfr(acc[4 * g + 3]) * sc)};
    }
  }
  unsigned cur = sb + 49152, nxt = sb, fre = sb + 24576;
  const unsigned ka = l * 16, kb = 1024 + l * 8, va = 1536 + hh * 384 + (m < 20 ? m : 20) * 16;
  WAITV(0); BAR();
#pragma unroll
  for (int j = 0; j < 3; j++) glds16(src + 24576 + j * 1024, sb + w * 3072 + j * 1024);
  auto qrd = [&](int qd) { return ldsr64(sb + (qd < 32 ? 40960 : 73728 - 8192) + qd * 256 + m * 8); };
  const u32x2 qa = qrd(w * 5 + hh), qc = qrd(w * 5 + 2 + hh), qd = qrd(w * 5 + 4);
  const bf16x8 q1 = asbf(u32x4{qa[0], qa[1], qc[0], qc[1]});
  u32x4 q2 = {hh ? 0u : qd[0], hh ? 0u : qd[1], 0u, 0u};
#define EX(v) __builtin_amdgcn_exp2f(v)
#define TR2(a, b) __builtin_amdgcn_perm(__float_as_uint(b), __float_as_uint(a), 0x07060302u)
  const f32x16 zero = {};
  u32x4 pb = {0u, 0u, 0u, 0u};
  float e0 = 0.f, e1 = 0.f, e2 = 0.f, e3 = 0.f;
  __builtin_amdgcn_s_setprio(2);
  bf16x8 k1 = ldsr128(cur + ka), v0 = ldsr128(cur + va), v1 = ldsr128(cur + va + 768);
  u32x4 k2 = {0u, 0u, 0u, 0u};
  { u32x2 t2 = ldsr64(cur + kb); k2[0] = t2[0]; k2[1] = t2[1]; }
  f32x16 s = mma32(asbf(k2), asbf(q2), mma32(k1, q1, zero)), o = {};
  k1 = ldsr128(cur + 3072 + ka);
  { u32x2 t2 = ldsr64(cur + 3072 + kb); k2[0] = t2[0]; k2[1] = t2[1]; }
  unsigned slot;
  float mref = split_ref(xhalf_max(max16(s)) + 60.f, slot);
  if (hh) q2[0] = slot;
#pragma unroll
  for (int j = 0; j < 16; j++) s[j] -= mref;
  for (int c = 0; c < 8; c++) {
    const unsigned ck = cur + ka, ck2 = cur + kb, cv = cur + va, nk = nxt + ka, nk2 = nxt + kb, nv = nxt + va;
#pragma unroll
    for (int t = 0; t < 8; t++) {
      if (t == 6) {
        WAITV(0); BAR();
        if (c < 6) {
#pragma unroll
          for (int j = 0; j < 3; j++) glds16(src + (c + 2) * 24576 + j * 1024, __builtin_amdgcn_readfirstlane(fre) + w * 3072 + j * 1024);
        }
      }
      if (t == 3) {
        if (__builtin_expect(__any(o[12] > 1024.f), 0)) {
          pb[2] = TR2(e0, e1); pb[3] = TR2(e2, e3);
          o = mma32(v1, asbf(pb), o);
          pb = u32x4{0u, 0u, 0u, 0u}; e0 = e1 = e2 = e3 = 0.f;
          float lm = xhalf_max(o[12]);
          int e = __builtin_amdgcn_frexp_expf(lm) + 59;
          float mn = split_ref(mref + (float)(e > 0 ? (e < 100 ? e : 100) : 0), slot);
          float dl = mn - mref, f = EX(-dl);
          mref = mn;
          if (hh) q2[0] = slot;
#pragma unroll
          for (int j = 0; j < 16; j++) { o[j] *= f; s[j] -= dl; }
        }
      }
      u32x4 pa;
#define SLOT(i, P0, P1) { float n0 = EX(s[i]), n1 = EX(s[i + 1]); SB(); P0 = TR2(e0, e1); SB(); float n2 = EX(s[i + 2]); SB(); \
                          P1 = TR2(e2, e3); SB(); e3 = EX(s[i + 3]); e0 = n0; e1 = n1; e2 = n2; SB(); }
      SB();
      f32x16 sn = mma32(k1, q1, zero);
      SB();
      k1 = ldsr128(t < 6 ? ck + (t + 2) * 3072 : nk + (t - 6) * 3072);
      SLOT(0, pb[2], pb[3])
      sn = mma32(asbf(k2), asbf(q2), sn);
      SB();
      { u32x2 t2 = ldsr64(t < 6 ? ck2 + (t + 2) * 3072 : nk2 + (t - 6) * 3072); k2[0] = t2[0]; k2[1] = t2[1]; }
      SLOT(4, pa[0], pa[1])
      o = mma32(v1, asbf(pb), o);
      SB();
      v1 = ldsr128(cv + t * 3072 + 768);
      SLOT(8, pa[2], pa[3])
      o = mma32(v0, asbf(pa), o);
      SB();
      v0 = ldsr128(t < 7 ? cv + (t + 1) * 3072 : nv);
      SLOT(12, pb[0], pb[1])
      s = sn;
    }
    unsigned tmp = cur; cur = nxt; nxt = fre; fre = tmp;
  }
  pb[2] = TR2(e0, e1); pb[3] = TR2(e2, e3);
  o = mma32(v1, asbf(pb), o);
  float chk = o[12];
#pragma unroll
  for (int j = 0; j < 12; j++) chk = fmaxf(chk, fabsf((hh && j >= 8) ? 0.f : o[j]) );
  bool bad = !(chk < 1e37f) || !(o[12] > 0.f);
#pragma unroll
  for (int j = 0; j < 13; j++) bad = bad || ((hh && j >= 8 && j < 12) ? false : (o[j] != o[j]));
  if (__builtin_expect(__any(bad), 0)) {
    if (hh) q2[0] = 0u;
    float mx = -INFINITY;
    o = f32x16{};
    for (int t = 0; t < 64; t++) {
      const char* tb = kvg + t * 3072;
      u32x2 k2 = *(const u32x2*)(tb + kb);
      f32x16 z = {};
      z = mma32(*(const bf16x8*)(tb + ka), q1, z);
      z = mma32(asbf(u32x4{k2[0], k2[1], 0u, 0u}), asbf(q2), z);
      float mn = fmaxf(mx, xhalf_max(max16(z)));
      float f = __builtin_amdgcn_exp2f(mx - mn);
      mx = mn;
#pragma unroll
      for (int j = 0; j < 16; j++) { o[j] *= f; z[j] -= mn; }
      bf16x8 p0, p1;
      packP(z, p0, p1);
      o = mma32(*(const bf16x8*)(tb + va), p0, o);
      o = mma32(*(const bf16x8*)(tb + va + 768), p1, o);
    }
  }
  const float inv = 1.f / o[12];
  char* ab = ws + OFF_A + (size_t)(row >> 4) * (KS * 1024) + (row & 15) * 16;
#pragma unroll
  for (int g = 0; g < 3; g++) {
    if (g < 2 || !hh) {
      const int k = head * 20 + 8 * g + 4 * hh;
      u32x2 ov = {pk2(o[4 * g] * inv, o[4 * g + 1] * inv), pk2(o[4 * g + 2] * inv, o[4 * g + 3] * inv)};
      *(u32x2*)(ab + (k >> 3) * 256 + ((k >> 2) & 1) * 8) = ov;
    }
  }
}

__device__ __forceinline__ void gu_body(int id, const u16* __restrict__ wg, const u16* __restrict__ wu, char* __restrict__ ws, unsigned sb) {
  const int tid = threadIdx.x, l = tid & 63, w = __builtin_amdgcn_readfirstlane(tid >> 6);
  const int hh = l >> 5, m = l & 31;
  const int T = ((id >> 1) & 3) * 27 + (id >> 3), nt = T >> 2, mt = (id & 1) * 4 + (T & 3);
  const char* xs = ws + OFF_XN + (size_t)(mt * 8 + w) * 40960 + l * 16;
  const u16* wsrc = ((w & 1) ? wu : wg) + (size_t)((w >> 1) * 8 + ((l & 31) >> 2)) * F_ + nt * 64 + (l >> 5) * 32 + (l & 3) * 8;
  auto issue = [&](int st, unsigned buf) {
    glds16(xs + st * 2048, buf + w * 2048);
    glds16(xs + st * 2048 + 1024, buf + w * 2048 + 1024);
    glds16(wsrc + (size_t)st * 32 * F_, buf + 16384 + w * 1024);
  };
  issue(0, sb); issue(1, sb + 24576);
  const int wm = w & 3, wn = w >> 2;
  const unsigned xa = wm * 4096 + l * 16;
  const unsigned wa = 16384 + hh * 2048 + wn * 512 + ((l & 15) >> 2) * 64 + ((l >> 4) & 1) * 32 + (l & 3) * 8;
  f32x16 acc[2][2] = {};
  unsigned cur = sb, nxt = sb + 24576, fre = sb + 49152;
  for (int st = 0; st < 20; st++) {
    if (st < 19) WAITV(3); else WAITV(0);
    BAR();
    if (st < 18) issue(st + 2, __builtin_amdgcn_readfirstlane(fre));
#pragma unroll
    for (int s = 0; s < 2; s++) {
      bf16x8 x0 = ldsr128(cur + xa + s * 1024), x1 = ldsr128(cur + xa + 2048 + s * 1024);
      bf16x8 g = ldstr(cur + wa + s * 4096, cur + wa + s * 4096 + 256);
      bf16x8 u = ldstr(cur + wa + s * 4096 + 1024, cur + wa + s * 4096 + 1024 + 256);
      acc[0][0] = mma32(g, x0, acc[0][0]); acc[0][1] = mma32(g, x1, acc[0][1]);
      acc[1][0] = mma32(u, x0, acc[1][0]); acc[1][1] = mma32(u, x1, acc[1][1]);
    }
    unsigned tmp = cur; cur = nxt; nxt = fre; fre = tmp;
  }
#pragma unroll
  for (int mb = 0; mb < 2; mb++) {
    const int row = mt * 256 + wm * 64 + mb * 32 + m;
    char* ab = ws + OFF_A + (size_t)(row >> 4) * (KS * 1024) + (20 + nt * 2 + wn) * 1024 + (row & 15) * 16 + hh * 8;
#pragma unroll
    for (int g = 0; g < 4; g++) {
      float h[4];
#pragma unroll
      for (int i = 0; i < 4; i++) {
        float gv = acc[0][mb][4 * g + i], uv = acc[1][mb][4 * g + i];
        h[i] = gv * __builtin_amdgcn_rcpf(1.f + __builtin_amdgcn_exp2f(gv * -1.4426950408889634f)) * uv;
      }
      *(u32x2*)(ab + g * 256) = u32x2{pk2(h[0], h[1]), pk2(h[2], h[3])};
    }
  }
}

#define GPRIO 0
#define GDEL 0
__global__ void __launch_bounds__(512, 4) k2_mix(const u16* __restrict__ wg, const u16* __restrict__ wu, const u16* __restrict__ wo,
                                                 const u16* __restrict__ wd, char* __restrict__ ws) {
  __shared__ __attribute__((aligned(16))) char smem[K2_LDS];
  const unsigned sb = (unsigned)(uintptr_t)(LAS char*)smem;
  const int b = blockIdx.x;
  if (b < N_ATT) attn_body(b, ws, sb);
  else if (b < N_ATT + N_GU) {
    __builtin_amdgcn_s_setprio(GPRIO);
    if (GDEL) __builtin_amdgcn_s_sleep(GDEL);
    gu_body(b - N_ATT, wg, wu, ws, sb);
  } else {
    __builtin_amdgcn_s_sleep(127); __builtin_amdgcn_s_sleep(64);
    for (int u = (b - N_ATT - N_GU) * 512 + threadIdx.x; u < 296 * 80; u += N_PACK * 512) pack_unit(u, wo, wd, wd, ws);
  }
}

constexpr int K3_DEPTH = 5;
__global__ void __launch_bounds__(256, 1) k3_out(const u16* __restrict__ x, const char* __restrict__ ws, u16* __restrict__ out) {
  __shared__ __attribute__((aligned(16))) float red[4 * 64 * 84];
  const int tid = threadIdx.x, l = tid & 63, w = __builtin_amdgcn_readfirstlane(tid >> 6);
  const int b = blockIdx.x, xcd = b & 7, j = b >> 3;
  const int mt = (xcd >> 1) * 8 + (j & 7), nt = (xcd & 1) * 4 + (j >> 3);
  const char* ap = ws + OFF_A + (size_t)mt * 4 * (KS * 1024) + l * 16;
  const char* bp = ws + OFF_W + (size_t)nt * 5 * (KS * 1024) + l * 16;
  bf16x8 fa[K3_DEPTH][4], fb[K3_DEPTH][5];
  f32x4 acc[4][5] = {};
  auto load = [&](int i, int slot) {
    int ks = w + 4 * i; ks = ks < KS ? ks : KS - 1;
#pragma unroll
    for (int t = 0; t < 5; t++) fb[slot][t] = *(const bf16x8*)(bp + (size_t)(t * KS + ks) * 1024);
#pragma unroll
    for (int t = 0; t < 4; t++) fa[slot][t] = *(const bf16x8*)(ap + (size_t)(t * KS + ks) * 1024);
  };
#pragma unroll
  for (int i = 0; i < K3_DEPTH; i++) { load(i, i); SB(); }
  const int er = tid >> 2, ec = (tid & 3) * 20;
  const size_t eo = (size_t)(mt * 64 + er) * D_ + nt * 80 + ec;
  u32x2 xr[5];
#pragma unroll
  for (int i = 0; i < 5; i++) xr[i] = *(const u32x2*)(x + eo + 4 * i);
  SB();
#pragma unroll
  for (int i = 0; i < 19; i++) {
    const int slot = i % K3_DEPTH;
    if (i < 18 || w < 2) {
#pragma unroll
      for (int a = 0; a < 4; a++)
#pragma unroll
        for (int t = 0; t < 5; t++) acc[a][t] = mma16(fb[slot][t], fa[slot][a], acc[a][t]);
    }
    SB();
    if (i + K3_DEPTH < 19) load(i + K3_DEPTH, slot);
    SB();
  }
  float* rw = red + w * (64 * 84);
#pragma unroll
  for (int a = 0; a < 4; a++)
#pragma unroll
    for (int t = 0; t < 5; t++) *(f32x4*)(rw + (a * 16 + (l & 15)) * 84 + t * 16 + (l >> 4) * 4) = acc[a][t];
  __syncthreads();
#pragma unroll
  for (int i = 0; i < 5; i++) {
    const float* rp = red + er * 84 + ec + 4 * i;
    f32x4 v = *(const f32x4*)rp + *(const f32x4*)(rp + 64 * 84) + *(const f32x4*)(rp + 2 * 64 * 84) + *(const f32x4*)(rp + 3 * 64 * 84);
    u32x2 o = {pk2(v[0] + lo16(xr[i][0]), v[1] + hi16(xr[i][0])), pk2(v[2] + lo16(xr[i][1]), v[3] + hi16(xr[i][1]))};
    *(u32x2*)(out + eo + 4 * i) = o;
  }
}

extern "C" void kernel_launch(void* const* d_in, const int* in_sizes, int n_in, void* d_out, int out_size, void* d_ws, size_t ws_size,
                              hipStream_t stream) {
  const u16* x = (const u16*)d_in[0];
  char* ws = (char*)d_ws;
  k1_ln_kv<<<256, 512, 0, stream>>>(x, (const u16*)d_in[2], (const u16*)d_in[3], (const u16*)d_in[1], (const u16*)d_in[6],
                                    (const u16*)d_in[7], (const u16*)d_in[8], (const u16*)d_in[4], ws);
  k2_mix<<<N_ATT + N_GU + N_PACK, 512, 0, stream>>>((const u16*)d_in[6], (const u16*)d_in[7], (const u16*)d_in[4], (const u16*)d_in[8], ws);
  k3_out<<<256, 256, 0, stream>>>(x, ws, (u16*)d_out);
}
```

```cpp
#include <hip/hip_runtime.h>
#include <hip/hip_bf16.h>
#include <stdint.h>

typedef __attribute__((ext_vector_type(8))) __bf16 bf16x8;
typedef __attribute__((ext_vector_type(2))) __bf16 bf16x2;
typedef __attribute__((ext_vector_type(4))) short s16x4;
typedef __attribute__((ext_vector_type(16))) float f32x16;
typedef __attribute__((ext_vector_type(4))) float f32x4;
typedef __attribute__((ext_vector_type(2))) float f32x2;
typedef __attribute__((ext_vector_type(4))) unsigned u32x4;
typedef __attribute__((ext_vector_type(2))) unsigned u32x2;
#define LAS __attribute__((address_space(3)))
typedef unsigned short u16;

constexpr int S_ = 2048, D_ = 640, F_ = 1728;
constexpr int KS = 74;
constexpr size_t OFF_XN = 0;
constexpr size_t OFF_Q = OFF_XN + 2621440;
constexpr size_t OFF_KV = OFF_Q + 819200;
constexpr size_t OFF_A = OFF_KV + 196608;
constexpr size_t OFF_W = OFF_A + 9699328;

__device__ __forceinline__ void glds16(const void* g, unsigned lds) {
  unsigned keep;
  asm volatile("s_mov_b32 %0, m0\n\ts_mov_b32 m0, %2\n\ts_nop 0\n\tglobal_load_lds_dwordx4 %1, off\n\ts_mov_b32 m0, %0"
               : "=&s"(keep) : "v"(g), "s"(lds) : "memory");
}
__device__ __forceinline__ unsigned pk2(float a, float b) {
  bf16x2 v = {(__bf16)a, (__bf16)b};
  return __builtin_bit_cast(unsigned, v);
}
__device__ __forceinline__ float bfr(float a) { return (float)(__bf16)a; }
__device__ __forceinline__ float lo16(unsigned u) { return __uint_as_float(u << 16); }
__device__ __forceinline__ float hi16(unsigned u) { return __uint_as_float(u & 0xffff0000u); }
__device__ __forceinline__ bf16x8 ldsr128(unsigned a) { return *(const LAS bf16x8*)(uintptr_t)a; }
__device__ __forceinline__ u32x2 ldsr64(unsigned a) { return *(const LAS u32x2*)(uintptr_t)a; }
__device__ __forceinline__ bf16x8 ldstr(unsigned a0, unsigned a1) {
  s16x4 x = __builtin_amdgcn_ds_read_tr16_b64_v4i16((LAS s16x4*)(uintptr_t)a0);
  s16x4 y = __builtin_amdgcn_ds_read_tr16_b64_v4i16((LAS s16x4*)(uintptr_t)a1);
  u32x2 xa = __builtin_bit_cast(u32x2, x), ya = __builtin_bit_cast(u32x2, y);
  u32x4 r = {xa[0], xa[1], ya[0], ya[1]};
  return __builtin_bit_cast(bf16x8, r);
}
__device__ __forceinline__ f32x16 mma32(bf16x8 a, bf16x8 b, f32x16 c) { return __builtin_amdgcn_mfma_f32_32x32x16_bf16(a, b, c, 0, 0, 0); }
__device__ __forceinline__ f32x4 mma16(bf16x8 a, bf16x8 b, f32x4 c) { return __builtin_amdgcn_mfma_f32_16x16x32_bf16(a, b, c, 0, 0, 0); }
__device__ __forceinline__ f32x16 mma32k8(u32x4 a, u32x4 b, f32x16 c) {
  const u32x2 a2 = {a[0], a[1]}, b2 = {b[0], b[1]};
  return __builtin_amdgcn_mfma_f32_32x32x8bf16_1k(__builtin_bit_cast(s16x4, a2), __builtin_bit_cast(s16x4, b2), c, 0, 0, 0);
}
__device__ __forceinline__ bf16x8 asbf(u32x4 v) { return __builtin_bit_cast(bf16x8, v); }
__device__ __forceinline__ float xhalf_max(float v) {
  auto r = __builtin_amdgcn_permlane32_swap(__float_as_uint(v), __float_as_uint(v), false, false);
  return fmaxf(__uint_as_float(r[0]), __uint_as_float(r[1]));
}
#define WAITV(n) asm volatile("s_waitcnt vmcnt(" #n ")" ::: "memory")
#define BAR() asm volatile("s_waitcnt lgkmcnt(0)\n\ts_barrier" ::: "memory")

#define SB() __builtin_amdgcn_sched_barrier(0)
__device__ __forceinline__ float row16_sum(float v) {
  v += __builtin_bit_cast(float, __builtin_amdgcn_update_dpp(0, __builtin_bit_cast(int, v), 0x128, 0xf, 0xf, false));
  v += __builtin_bit_cast(float, __builtin_amdgcn_update_dpp(0, __builtin_bit_cast(int, v), 0x124, 0xf, 0xf, false));
  v += __builtin_bit_cast(float, __builtin_amdgcn_update_dpp(0, __builtin_bit_cast(int, v), 0x122, 0xf, 0xf, false));
  v += __builtin_bit_cast(float, __builtin_amdgcn_update_dpp(0, __builtin_bit_cast(int, v), 0x121, 0xf, 0xf, false));
  return v;
}
__device__ __forceinline__ void pack_unit(int u, const u16* __restrict__ wo, const u16* __restrict__ wd, const u16* __restrict__ wq,
                                          char* __restrict__ ws) {
  const int ko = u / 80, no = u - ko * 80;
  const u16* src = (ko < 80 ? wo + (size_t)ko * 8 * D_ : (ko < 296 ? wd + (size_t)(ko - 80) * 8 * D_ : wq + (size_t)(ko - 296) * 8 * D_)) + no * 8;
  u32x4 r[8];
#pragma unroll
  for (int i = 0; i < 8; i++) r[i] = *(const u32x4*)(src + i * D_);
  char* dst = ko < 296 ? ws + OFF_W + (size_t)(no >> 1) * (KS * 1024) + ko * 256 + (no & 1) * 128
                       : ws + OFF_Q + (no >> 2) * 40960 + (ko - 296) * 512 + (no & 3) * 128;
#pragma unroll
  for (int j = 0; j < 4; j++) {
    u32x4 e, o;
#pragma unroll
    for (int i = 0; i < 4; i++) {
      e[i] = __builtin_amdgcn_perm(r[2 * i + 1][j], r[2 * i][j], 0x05040100u);
      o[i] = __builtin_amdgcn_perm(r[2 * i + 1][j], r[2 * i][j], 0x07060302u);
    }
    *(u32x4*)(dst + j * 32) = e;
    *(u32x4*)(dst + j * 32 + 16) = o;
  }
}
constexpr int K1_KV = 40960, K1_RED = K1_KV + 25600 + 128, K1_LDS = K1_RED + 3 * 4096;
__global__ void __launch_bounds__(512, 2) k1_ln_kv(const u16* __restrict__ x, const u16* __restrict__ wk, const u16* __restrict__ wv,
                                                   const u16* __restrict__ wq, char* __restrict__ ws) {
  __shared__ __attribute__((aligned(16))) char smem[K1_LDS];
  const unsigned sb = (unsigned)(uintptr_t)(LAS char*)smem;
  const int tid = threadIdx.x, l = tid & 63, w = __builtin_amdgcn_readfirstlane(tid >> 6);
  const int b = blockIdx.x;
  if (b >= 128) {
    if (tid < 50) pack_unit(296 * 80 + (b - 128) * 50 + tid, wq, wq, wq, ws);
    return;
  }
  const int cg = b & 1, rb = b >> 1, r = tid >> 4, p = tid & 15;
  u32x4 xv[5];
  const u16* xp = x + (size_t)(rb * 32 + r) * D_ + p * 8;
#pragma unroll
  for (int i = 0; i < 5; i++) xv[i] = *(const u32x4*)(xp + i * 128);
  SB();
  {
    const u16* src = (cg == 0 ? wk : wv) + l * 8;
#pragma unroll
    for (int j = 0; j < 3; j++) glds16(src + (w * 3 + j) * 512, sb + K1_KV + (w * 3 + j) * 1024);
    if (w == 7) glds16(src + 24 * 512, sb + K1_KV + 24 * 1024);
  }
  float s = 0.f, s1 = 0.f;
  const bf16x2 one2 = {(__bf16)1.f, (__bf16)1.f};
#pragma unroll
  for (int i = 0; i < 5; i++)
#pragma unroll
    for (int j = 0; j < 4; j += 2) {
      const unsigned u0 = xv[i][j], u1 = xv[i][j + 1];
      s = __builtin_amdgcn_fdot2_f32_bf16(__builtin_bit_cast(bf16x2, u0), one2, s, false);
      s1 = __builtin_amdgcn_fdot2_f32_bf16(__builtin_bit_cast(bf16x2, u1), one2, s1, false);
    }
  const float mean = row16_sum(s + s1) * (1.f / D_), mb = bfr(mean);
  f32x2 v[20], qa = {0.f, 0.f}, qb = {0.f, 0.f};
#pragma unroll
  for (int i = 0; i < 20; i++) {
    v[i] = f32x2{lo16(xv[i >> 2][i & 3]), hi16(xv[i >> 2][i & 3])} - f32x2{mb, mb};
    if (i & 1) qb += v[i] * v[i]; else qa += v[i] * v[i];
  }
  qa += qb;
  const float dm = mean - mb, vb = bfr(fmaxf(row16_sum(qa[0] + qa[1]) * (1.f / D_) - dm * dm, 0.f));
  const float rs = __builtin_amdgcn_rcpf(bfr(__builtin_amdgcn_sqrtf(bfr(vb + 1e-5f))));
  char* xnf = ws + OFF_XN + (size_t)rb * 40960;
  WAITV(0);
#pragma unroll
  for (int i = 0; i < 5; i++) {
    u32x4 o;
#pragma unroll
    for (int j = 0; j < 4; j++) {
      const unsigned d = pk2(v[i * 4 + j][0], v[i * 4 + j][1]);
      const f32x2 t = f32x2{lo16(d), hi16(d)} * f32x2{rs, rs};
      o[j] = pk2(t[0], t[1]);
    }
    const int c = p + 16 * i;
    *(LAS u32x4*)(uintptr_t)(sb + c * 512 + ((r ^ (c & 7)) << 4)) = o;
    if (i == 4 ? ((p >> 3) == cg) : ((i >> 1) == cg)) *(u32x4*)(xnf + c * 512 + r * 16) = o;
  }
  BAR();
  const int hh = l >> 5, m = l & 31;
  f32x16 acc = {};
  if (w < 4) {
    const unsigned ta = sb + K1_KV + (hh * 8 + ((l & 15) >> 2)) * 40 + ((l >> 4) & 1) * 32 + (l & 3) * 8 + w * 6400;
    bf16x8 xf[10], wf[10];
#pragma unroll
    for (int k = 0; k < 10; k++) {
      const int c = (w * 10 + k) * 2 + hh;
      xf[k] = ldsr128(sb + c * 512 + ((m ^ (c & 7)) << 4));
      wf[k] = ldstr(ta + k * 640, ta + k * 640 + 160);
    }
    if (cg == 0) {
#pragma unroll
      for (int k = 0; k < 10; k++) acc = mma32(wf[k], xf[k], acc);
    } else {
#pragma unroll
      for (int k = 0; k < 10; k++) acc = mma32(xf[k], wf[k], acc);
    }
    if (w > 0) {
#pragma unroll
      for (int g = 0; g < 4; g++)
        *(LAS f32x4*)(uintptr_t)(sb + K1_RED + (w - 1) * 4096 + g * 1024 + l * 16) = f32x4{acc[4 * g], acc[4 * g + 1], acc[4 * g + 2], acc[4 * g + 3]};
    }
  }
  BAR();
  if (w == 0) {
#pragma unroll
    for (int q = 0; q < 3; q++)
#pragma unroll
      for (int g = 0; g < 4; g++) {
        f32x4 t = *(const LAS f32x4*)(uintptr_t)(sb + K1_RED + q * 4096 + g * 1024 + l * 16);
#pragma unroll
        for (int i = 0; i < 4; i++) acc[4 * g + i] += t[i];
      }
    char* tile = ws + OFF_KV + (size_t)rb * 3072;
    if (cg == 0) {
      u32x4 o = {pk2(acc[0], acc[1]), pk2(acc[2], acc[3]), pk2(acc[4], acc[5]), pk2(acc[6], acc[7])};
      *(u32x4*)(tile + l * 16) = o;
      u32x2 o2 = {hh ? 0x3f803f80u : pk2(acc[8], acc[9]), hh ? 0u : pk2(acc[10], acc[11])};
      *(u32x2*)(tile + 1024 + l * 8) = o2;
    } else if (m < 24) {
#pragma unroll
      for (int s2 = 0; s2 < 2; s2++) {
        u32x4 o = {pk2(acc[8 * s2], acc[8 * s2 + 1]), pk2(acc[8 * s2 + 2], acc[8 * s2 + 3]), pk2(acc[8 * s2 + 4], acc[8 * s2 + 5]), pk2(acc[8 * s2 + 6], acc[8 * s2 + 7])};
        if (m >= 20) o = u32x4{0x3f803f80u, 0x3f803f80u, 0x3f803f80u, 0x3f803f80u};
        *(u32x4*)(tile + 1536 + (s2 * 2 + hh) * 384 + m * 16) = o;
      }
    }
  }
}

constexpr int K2_LDS = 3 * 24576 + 2048;
constexpr int QD = 20;
constexpr int N_ATT = 256, N_GU = 216, N_PACK = 40;

__device__ __forceinline__ void packP(const f32x16& s, bf16x8& p0, bf16x8& p1) {
  u32x4 a, b;
#pragma unroll
  for (int j = 0; j < 4; j++) {
    a[j] = pk2(__builtin_amdgcn_exp2f(s[2 * j]), __builtin_amdgcn_exp2f(s[2 * j + 1]));
    b[j] = pk2(__builtin_amdgcn_exp2f(s[8 + 2 * j]), __builtin_amdgcn_exp2f(s[8 + 2 * j + 1]));
  }
  p0 = asbf(a); p1 = asbf(b);
}
__device__ __forceinline__ float max16(const f32x16& s) {
  float a = fmaxf(fmaxf(s[0], s[1]), fmaxf(s[2], s[3]));
#pragma unroll
  for (int j = 4; j < 16; j += 2) a = fmaxf(a, fmaxf(s[j], s[j + 1]));
  return a;
}
__device__ __forceinline__ float split_ref(float mv, unsigned& slot) {
  float hi = bfr(mv), lo = bfr(mv - hi);
  slot = pk2(-hi, -lo);
  return hi + lo;
}

__device__ __forceinline__ void attn_body(int a, char* __restrict__ ws, unsigned sb) {
  const int tid = threadIdx.x, l = tid & 63, w = __builtin_amdgcn_readfirstlane(tid >> 6);
  const int hh = l >> 5, m = l & 31;
  const int qb = (a & 1) * 32 + (a >> 3), hg = (a >> 1) & 3, head = hg * 8 + w;
  __builtin_amdgcn_s_setprio(3);
  const char* kvg = ws + OFF_KV;
  const char* src = kvg + w * 3072 + l * 16;
  const int row = qb * 32 + m;
  {
    const char* wp = ws + OFF_Q + (hg * 5 + (w < 5 ? w : 4)) * 40960 + l * 16;
    bf16x8 wf[QD], xf[2][2];
    if (w < 5) {
#pragma unroll
      for (int k = 0; k < QD; k++) wf[k] = *(const bf16x8*)(wp + k * 1024);
    }
    SB();
#pragma unroll
    for (int j = 0; j < 5; j++) glds16(ws + OFF_XN + (size_t)qb * 40960 + (w * 5 + j) * 1024 + l * 16, sb + (w * 5 + j) * 1024);
#pragma unroll
    for (int j = 0; j < 3; j++) glds16(src + j * 1024, sb + 49152 + w * 3072 + j * 1024);
    WAITV(3); BAR();
    if (w < 5) {
      f32x16 acc = {};
#pragma unroll
      for (int i = 0; i < 2; i++) xf[0][i] = ldsr128(sb + i * 1024 + l * 16);
#pragma unroll
      for (int g = 0; g < 20; g++) {
        SB();
        if (g < 19) {
#pragma unroll
          for (int i = 0; i < 2; i++) xf[(g + 1) & 1][i] = ldsr128(sb + (g * 2 + 2 + i) * 1024 + l * 16);
        }
        SB();
#pragma unroll
        for (int i = 0; i < 2; i++) {
          const int k = g * 2 + i;
          acc = mma32(wf[k % QD], xf[g & 1][i], acc);
          SB();
          if (k + QD < 40) wf[k % QD] = *(const bf16x8*)(wp + (k + QD) * 1024);
        }
      }
      const float sc = 0.22360679774997896f * 1.4426950408889634f;
      const unsigned qw = sb + (w < 4 ? 40960 + w * 2048 : 73728) + hh * 256 + m * 8;
#pragma unroll
      for (int g = 0; g < 4; g++)
        *(LAS u32x2*)(uintptr_t)(qw + g * 512) = u32x2{pk2(bfr(acc[4 * g]) * sc, bfr(acc[4 * g + 1]) * sc), pk2(bfr(acc[4 * g + 2]) * sc, bfr(acc[4 * g + 3]) * sc)};
    }
  }
  unsigned cur = sb + 49152, nxt = sb, fre = sb + 24576;
  const unsigned ka = l * 16, kb = 1024 + l * 8, va = 1536 + hh * 384 + (m < 20 ? m : 20) * 16;
  __builtin_amdgcn_s_waitcnt(0x0F70);
  WAITV(0); BAR();
#pragma unroll
  for (int j = 0; j < 3; j++) glds16(src + 24576 + j * 1024, sb + w * 3072 + j * 1024);
  auto qrd = [&](int qd) { return ldsr64(sb + (qd < 32 ? 40960 : 73728 - 8192) + qd * 256 + m * 8); };
  const u32x2 qa = qrd(w * 5 + hh), qc = qrd(w * 5 + 2 + hh), qd = qrd(w * 5 + 4);
  const bf16x8 q1 = asbf(u32x4{qa[0], qa[1], qc[0], qc[1]});
  u32x4 q2 = {hh ? 0u : qd[0], hh ? 0u : qd[1], 0u, 0u};
#define EX(v) __builtin_amdgcn_exp2f(v)
#define TR2(a, b) __builtin_amdgcn_perm(__float_as_uint(b), __float_as_uint(a), 0x07060302u)
  const f32x16 zero = {};
  u32x4 pb = {0u, 0u, 0u, 0u};
  float e0 = 0.f, e1 = 0.f, e2 = 0.f, e3 = 0.f;
  __builtin_amdgcn_s_setprio(2);
  bf16x8 k1 = ldsr128(cur + ka), v0 = ldsr128(cur + va), v1 = ldsr128(cur + va + 768);
  u32x4 k2 = {0u, 0u, 0u, 0u};
  { u32x2 t2 = ldsr64(cur + kb); k2[0] = t2[0]; k2[1] = t2[1]; }
  f32x16 s = mma32k8(k2, q2, mma32(k1, q1, zero)), o = {};
  k1 = ldsr128(cur + 3072 + ka);
  { u32x2 t2 = ldsr64(cur + 3072 + kb); k2[0] = t2[0]; k2[1] = t2[1]; }
  unsigned slot;
  float mref = split_ref(xhalf_max(max16(s)) + 60.f, slot);
  if (hh) q2[0] = slot;
#pragma unroll
  for (int j = 0; j < 16; j++) s[j] -= mref;
  for (int c = 0; c < 8; c++) {
    const unsigned ck = cur + ka, ck2 = cur + kb, cv = cur + va, nk = nxt + ka, nk2 = nxt + kb, nv = nxt + va;
#pragma unroll
    for (int t = 0; t < 8; t++) {
      if (t == 6) {
        WAITV(0); BAR();
        if (c < 6) {
#pragma unroll
          for (int j = 0; j < 3; j++) glds16(src + (c + 2) * 24576 + j * 1024, __builtin_amdgcn_readfirstlane(fre) + w * 3072 + j * 1024);
        }
      }
      if (t == 3) {
        if (__builtin_expect(__any(o[12] > 1024.f), 0)) {
          pb[2] = TR2(e0, e1); pb[3] = TR2(e2, e3);
          o = mma32(v1, asbf(pb), o);
          pb = u32x4{0u, 0u, 0u, 0u}; e0 = e1 = e2 = e3 = 0.f;
          float lm = xhalf_max(o[12]);
          int e = __builtin_amdgcn_frexp_expf(lm) + 59;
          float mn = split_ref(mref + (float)(e > 0 ? (e < 100 ? e : 100) : 0), slot);
          float dl = mn - mref, f = EX(-dl);
          mref = mn;
          if (hh) q2[0] = slot;
#pragma unroll
          for (int j = 0; j < 16; j++) { o[j] *= f; s[j] -= dl; }
        }
      }
      u32x4 pa;
#define SLOT(i, P0, P1) { float n0 = EX(s[i]), n1 = EX(s[i + 1]); SB(); P0 = TR2(e0, e1); SB(); float n2 = EX(s[i + 2]); SB(); \
                          P1 = TR2(e2, e3); SB(); e3 = EX(s[i + 3]); e0 = n0; e1 = n1; e2 = n2; SB(); }
      SB();
      f32x16 sn = mma32(k1, q1, zero);
      SB();
      k1 = ldsr128(t < 6 ? ck + (t + 2) * 3072 : nk + (t - 6) * 3072);
      SLOT(0, pb[2], pb[3])
      sn = mma32k8(k2, q2, sn);
      SB();
      { u32x2 t2 = ldsr64(t < 6 ? ck2 + (t + 2) * 3072 : nk2 + (t - 6) * 3072); k2[0] = t2[0]; k2[1] = t2[1]; }
      SLOT(4, pa[0], pa[1])
      o = mma32(v1, asbf(pb), o);
      SB();
      v1 = ldsr128(cv + t * 3072 + 768);
      SLOT(8, pa[2], pa[3])
      o = mma32(v0, asbf(pa), o);
      SB();
      v0 = ldsr128(t < 7 ? cv + (t + 1) * 3072 : nv);
      SLOT(12, pb[0], pb[1])
      s = sn;
    }
    unsigned tmp = cur; cur = nxt; nxt = fre; fre = tmp;
  }
  pb[2] = TR2(e0, e1); pb[3] = TR2(e2, e3);
  o = mma32(v1, asbf(pb), o);
  float chk = o[12];
#pragma unroll
  for (int j = 0; j < 12; j++) chk = fmaxf(chk, fabsf((hh && j >= 8) ? 0.f : o[j]) );
  bool bad = !(chk < 1e37f) || !(o[12] > 0.f);
#pragma unroll
  for (int j = 0; j < 13; j++) bad = bad || ((hh && j >= 8 && j < 12) ? false : (o[j] != o[j]));
  if (__builtin_expect(__any(bad), 0)) {
    if (hh) q2[0] = 0u;
    float mx = -INFINITY;
    o = f32x16{};
    for (int t = 0; t < 64; t++) {
      const char* tb = kvg + t * 3072;
      u32x2 k2 = *(const u32x2*)(tb + kb);
      f32x16 z = {};
      z = mma32(*(const bf16x8*)(tb + ka), q1, z);
      z = mma32(asbf(u32x4{k2[0], k2[1], 0u, 0u}), asbf(q2), z);
      float mn = fmaxf(mx, xhalf_max(max16(z)));
      float f = __builtin_amdgcn_exp2f(mx - mn);
      mx = mn;
#pragma unroll
      for (int j = 0; j < 16; j++) { o[j] *= f; z[j] -= mn; }
      bf16x8 p0, p1;
      packP(z, p0, p1);
      o = mma32(*(const bf16x8*)(tb + va), p0, o);
      o = mma32(*(const bf16x8*)(tb + va + 768), p1, o);
    }
  }
  const float inv = 1.f / o[12];
  char* ab = ws + OFF_A + (size_t)(row >> 4) * (KS * 1024) + (row & 15) * 16;
#pragma unroll
  for (int g = 0; g < 3; g++) {
    if (g < 2 || !hh) {
      const int k = head * 20 + 8 * g + 4 * hh;
      u32x2 ov = {pk2(o[4 * g] * inv, o[4 * g + 1] * inv), pk2(o[4 * g + 2] * inv, o[4 * g + 3] * inv)};
      *(u32x2*)(ab + (k >> 3) * 256 + ((k >> 2) & 1) * 8) = ov;
    }
  }
}

__device__ __forceinline__ void gu_body(int id, const u16* __restrict__ wg, const u16* __restrict__ wu, char* __restrict__ ws, unsigned sb) {
  const int tid = threadIdx.x, l = tid & 63, w = __builtin_amdgcn_readfirstlane(tid >> 6);
  const int hh = l >> 5, m = l & 31;
  const int T = ((id >> 1) & 3) * 27 + (id >> 3), nt = T >> 2, mt = (id & 1) * 4 + (T & 3);
  const char* xs = ws + OFF_XN + (size_t)(mt * 8 + w) * 40960 + l * 16;
  const u16* wsrc = ((w & 1) ? wu : wg) + (size_t)((w >> 1) * 8 + ((l & 31) >> 2)) * F_ + nt * 64 + (l >> 5) * 32 + (l & 3) * 8;
  auto issue = [&](int st, unsigned buf) {
    glds16(xs + st * 2048, buf + w * 2048);
    glds16(xs + st * 2048 + 1024, buf + w * 2048 + 1024);
    glds16(wsrc + (size_t)st * 32 * F_, buf + 16384 + w * 1024);
  };
  issue(0, sb); issue(1, sb + 24576);
  const int wm = w & 3, wn = w >> 2;
  const unsigned xa = wm * 4096 + l * 16;
  const unsigned wa = 16384 + hh * 2048 + wn * 512 + ((l & 15) >> 2) * 64 + ((l >> 4) & 1) * 32 + (l & 3) * 8;
  f32x16 acc[2][2] = {};
  unsigned cur = sb, nxt = sb + 24576, fre = sb + 49152;
  for (int st = 0; st < 20; st++) {
    if (st < 19) WAITV(3); else WAITV(0);
    BAR();
    if (st < 18) issue(st + 2, __builtin_amdgcn_readfirstlane(fre));
#pragma unroll
    for (int s = 0; s < 2; s++) {
      bf16x8 x0 = ldsr128(cur + xa + s * 1024), x1 = ldsr128(cur + xa + 2048 + s * 1024);
      bf16x8 g = ldstr(cur + wa + s * 4096, cur + wa + s * 4096 + 256);
      bf16x8 u = ldstr(cur + wa + s * 4096 + 1024, cur + wa + s * 4096 + 1024 + 256);
      acc[0][0] = mma32(g, x0, acc[0][0]); acc[0][1] = mma32(g, x1, acc[0][1]);
      acc[1][0] = mma32(u, x0, acc[1][0]); acc[1][1] = mma32(u, x1, acc[1][1]);
    }
    unsigned tmp = cur; cur = nxt; nxt = fre; fre = tmp;
  }
#pragma unroll
  for (int mb = 0; mb < 2; mb++) {
    const int row = mt * 256 + wm * 64 + mb * 32 + m;
    char* ab = ws + OFF_A + (size_t)(row >> 4) * (KS * 1024) + (20 + nt * 2 + wn) * 1024 + (row & 15) * 16 + hh * 8;
#pragma unroll
    for (int g = 0; g < 4; g++) {
      float h[4];
#pragma unroll
      for (int i = 0; i < 4; i++) {
        float gv = acc[0][mb][4 * g + i], uv = acc[1][mb][4 * g + i];
        h[i] = gv * __builtin_amdgcn_rcpf(1.f + __builtin_amdgcn_exp2f(gv * -1.4426950408889634f)) * uv;
      }
      *(u32x2*)(ab + g * 256) = u32x2{pk2(h[0], h[1]), pk2(h[2], h[3])};
    }
  }
}

__global__ void __launch_bounds__(512, 4) k2_mix(const u16* __restrict__ wg, const u16* __restrict__ wu, const u16* __restrict__ wo,
                                                 const u16* __restrict__ wd, char* __restrict__ ws) {
  __shared__ __attribute__((aligned(16))) char smem[K2_LDS];
  const unsigned sb = (unsigned)(uintptr_t)(LAS char*)smem;
  const int b = blockIdx.x;
  if (b < N_ATT) attn_body(b, ws, sb);
  else if (b < N_ATT + N_GU) gu_body(b - N_ATT, wg, wu, ws, sb);
  else {
    __builtin_amdgcn_s_sleep(127); __builtin_amdgcn_s_sleep(64);
    for (int u = (b - N_ATT - N_GU) * 512 + threadIdx.x; u < 296 * 80; u += N_PACK * 512) pack_unit(u, wo, wd, wd, ws);
  }
}

constexpr int K3_DEPTH = 5;
__global__ void __launch_bounds__(256, 1) k3_out(const u16* __restrict__ x, const char* __restrict__ ws, u16* __restrict__ out) {
  __shared__ __attribute__((aligned(16))) float red[4 * 64 * 84];
  const int tid = threadIdx.x, l = tid & 63, w = __builtin_amdgcn_readfirstlane(tid >> 6);
  const int b = blockIdx.x, xcd = b & 7, j = b >> 3;
  const int mt = (xcd >> 1) * 8 + (j & 7), nt = (xcd & 1) * 4 + (j >> 3);
  const char* ap = ws + OFF_A + (size_t)mt * 4 * (KS * 1024) + l * 16;
  const char* bp = ws + OFF_W + (size_t)nt * 5 * (KS * 1024) + l * 16;
  bf16x8 fa[K3_DEPTH][4], fb[K3_DEPTH][5];
  f32x4 acc[4][5] = {};
  auto load = [&](int i, int slot) {
    int ks = w + 4 * i; ks = ks < KS ? ks : KS - 1;
#pragma unroll
    for (int t = 0; t < 5; t++) fb[slot][t] = *(const bf16x8*)(bp + (size_t)(t * KS + ks) * 1024);
#pragma unroll
    for (int t = 0; t < 4; t++) fa[slot][t] = *(const bf16x8*)(ap + (size_t)(t * KS + ks) * 1024);
  };
#pragma unroll
  for (int i = 0; i < K3_DEPTH; i++) { load(i, i); SB(); }
  const int er = tid >> 2, ec = (tid & 3) * 20;
  const size_t eo = (size_t)(mt * 64 + er) * D_ + nt * 80 + ec;
  u32x2 xr[5];
#pragma unroll
  for (int i = 0; i < 5; i++) xr[i] = *(const u32x2*)(x + eo + 4 * i);
  SB();
#pragma unroll
  for (int i = 0; i < 19; i++) {
    const int slot = i % K3_DEPTH;
    if (i < 18 || w < 2) {
#pragma unroll
      for (int a = 0; a < 4; a++)
#pragma unroll
        for (int t = 0; t < 5; t++) acc[a][t] = mma16(fb[slot][t], fa[slot][a], acc[a][t]);
    }
    SB();
    if (i + K3_DEPTH < 19) load(i + K3_DEPTH, slot);
    SB();
  }
  float* rw = red + w * (64 * 84);
#pragma unroll
  for (int a = 0; a < 4; a++)
#pragma unroll
    for (int t = 0; t < 5; t++) *(f32x4*)(rw + (a * 16 + (l & 15)) * 84 + t * 16 + (l >> 4) * 4) = acc[a][t];
  __syncthreads();
#pragma unroll
  for (int i = 0; i < 5; i++) {
    const float* rp = red + er * 84 + ec + 4 * i;
    f32x4 v = *(const f32x4*)rp + *(const f32x4*)(rp + 64 * 84) + *(const f32x4*)(rp + 2 * 64 * 84) + *(const f32x4*)(rp + 3 * 64 * 84);
    u32x2 o = {pk2(v[0] + lo16(xr[i][0]), v[1] + hi16(xr[i][0])), pk2(v[2] + lo16(xr[i][1]), v[3] + hi16(xr[i][1]))};
    *(u32x2*)(out + eo + 4 * i) = o;
  }
}

extern "C" void kernel_launch(void* const* d_in, const int* in_sizes, int n_in, void* d_out, int out_size, void* d_ws, size_t ws_size,
                              hipStream_t stream) {
  const u16* x = (const u16*)d_in[0];
  char* ws = (char*)d_ws;
  k1_ln_kv<<<256, 512, 0, stream>>>(x, (const u16*)d_in[2], (const u16*)d_in[3], (const u16*)d_in[1], ws);
  k2_mix<<<N_ATT + N_GU + N_PACK, 512, 0, stream>>>((const u16*)d_in[6], (const u16*)d_in[7], (const u16*)d_in[4], (const u16*)d_in[8], ws);
  k3_out<<<256, 256, 0, stream>>>(x, ws, (u16*)d_out);
}
```
